```python
import torch

def _softmax(x, axis=-1):
    return torch.softmax(x, dim=axis)
def _gelu(x):
    return torch.nn.functional.gelu(x, approximate="tanh")
def _silu(x):
    return torch.nn.functional.silu(x)
def _sigmoid(x):
    return torch.sigmoid(x)
def _layernorm(x, axis=-1, eps=1e-5):
    m = x.mean(dim=axis, keepdim=True)
    v = x.var(dim=axis, keepdim=True, unbiased=False)
    return (x - m) / torch.sqrt(v + eps)
def _scatter_add(tgt, idx, src, axis=0):
    return tgt.clone().index_add_(axis, idx, src)
def _segment_sum(src, seg, n):
    out = torch.zeros((n,) + src.shape[1:], dtype=src.dtype)
    return out.index_add_(0, seg, src)
def _pad(x, pw):
    flat = tuple(v for pair in reversed(pw) for v in pair)
    return torch.nn.functional.pad(x, flat)

kernel_name = 'standalone__edge_featured_mpnn_mpnn__graph'

def setup_inputs():

    _w = (lambda a: (lambda t: t.to(torch.bfloat16) if t.is_floating_point() else t)(torch.as_tensor(a)))
    N, deg, D, De = 25000, 16, 128, max(0, 1)
    E = N * deg
    H = 2 * D
    return {
        "x": _w((lambda *s: torch.randn(*s, dtype=torch.bfloat16) * 0.05)(N, D)),
        "e_attr": _w((lambda *s: torch.randn(*s, dtype=torch.bfloat16) * 1.6)(E, De)),
        "src": _w((lambda lo, hi, *s: torch.randint(lo, hi, s))(0, N, E).to(torch.int64)),
        "dst": _w((lambda lo, hi, *s: torch.randint(lo, hi, s))(0, N, E).to(torch.int64)),
        "w1": _w((lambda *s: torch.randn(*s, dtype=torch.bfloat16) * 1.6)(2 * D + De, H) * (1.0 / (2 * D + De) ** 0.5)),
        "b1": _w((lambda *s: torch.randn(*s, dtype=torch.bfloat16) * 1.6)(H) * 0.01),
        "w2": _w((lambda *s: torch.randn(*s, dtype=torch.bfloat16) * 0.05)(H, D) * (1.0 / H ** 0.5)),
        "b2": _w((lambda *s: torch.randn(*s, dtype=torch.bfloat16) * 0.05)(D) * 0.01),
    }

def reference(x, e_attr, src, dst, w1, b1, w2, b2):
    N, D = x.shape
    h_src = x[src]
    h_dst = x[dst]
    m_in = torch.concatenate([h_src, h_dst, e_attr], axis=-1)
    h = _silu(m_in @ w1 + b1)
    msg = h @ w2 + b2
    return _segment_sum(msg, dst, N)
```

```cpp
#include <hip/hip_runtime.h>
#include <hip/hip_bf16.h>


typedef unsigned int u32;
typedef unsigned short u16;
typedef unsigned long long u64;
using bf16x8 = __attribute__((ext_vector_type(8))) short;
using f32x16 = __attribute__((ext_vector_type(16))) float;
using f32x2 = __attribute__((ext_vector_type(2))) float;
using u32x4 = __attribute__((ext_vector_type(4))) u32;
using u32x2 = __attribute__((ext_vector_type(2))) u32;
using bf16x2 = __attribute__((ext_vector_type(2))) __bf16;

#define DI __device__ __forceinline__
#define MFMA(a, b, c) __builtin_amdgcn_mfma_f32_32x32x16_bf16((a), (b), (c), 0, 0, 0)

constexpr int NN = 25000, NE = 400000;
constexpr int NB = 64, NPB = 391;
constexpr int NCH = 60, EPC = 6668;
constexpr int CAP = 160;
constexpr int NG1 = 196;
constexpr int LST = 49;
constexpr float LOG2E = 1.4426950408889634f, LN2 = 0.6931471805599453f;

constexpr size_t WS_T = 0;
constexpr size_t WS_S = WS_T + (size_t)4 * 2 * NN * 128;
constexpr size_t WS_R = WS_S + (size_t)NN * 512;
constexpr size_t WS_DEG = WS_R + (size_t)NB * NCH * CAP * 8;
constexpr size_t WS_W2 = WS_DEG + 100352;

DI float bf2f(u32 h) { return __uint_as_float(h << 16); }
DI u32 pk_bf16(float a, float b) {
  f32x2 v = {a, b};
  return __builtin_bit_cast(u32, __builtin_convertvector(v, bf16x2));
}

__global__ __launch_bounds__(512, 2) void k1(const u16* __restrict__ x, const u16* __restrict__ ea,
                                             const int* __restrict__ src, const int* __restrict__ dst,
                                             const u16* __restrict__ w1, const u16* __restrict__ b1,
                                             char* __restrict__ ws) {
  extern __shared__ __attribute__((aligned(16))) char lds[];
  const int tid = threadIdx.x, l = tid & 63, w = __builtin_amdgcn_readfirstlane(tid >> 6);
  if (blockIdx.x < NCH) {
    const int c = blockIdx.x, e0 = c * EPC, e1 = min(e0 + EPC, NE);
    u32x4 vd[4], vs[4];
    u32x2 ve[4];
#pragma unroll
    for (int i = 0; i < 4; ++i) {
      const int e = e0 + 4 * (tid + 512 * i);
      if (e < e1) {
        vd[i] = *(const u32x4*)(dst + e);
        vs[i] = *(const u32x4*)(src + e);
        ve[i] = *(const u32x2*)(ea + e);
      }
    }
    u32* cnt = (u32*)(lds + NB * CAP * 8);
    const u32x4 inv = {0u, 0xFFFFFFFFu, 0u, 0xFFFFFFFFu};
    for (int i = tid; i < NB * CAP / 2; i += 512) ((u32x4*)lds)[i] = inv;
    if (tid < NB) cnt[tid] = 0;
    __syncthreads();
#pragma unroll
    for (int i = 0; i < 4; ++i) {
      const int e = e0 + 4 * (tid + 512 * i);
      if (e < e1) {
#pragma unroll
        for (int j = 0; j < 4; ++j) {
          const u32 d = vd[i][j], b = d / NPB;
          const u32 r = atomicAdd(&cnt[b], 1u);
          const u32 eb = (ve[i][j >> 1] >> (16 * (j & 1))) & 0xFFFFu;
          if (r < CAP - 1) {
            u32x2 rec = {eb | (vs[i][j] << 16), d - b * NPB};
            *(u32x2*)(lds + (b * CAP + r) * 8) = rec;
          }
        }
      }
    }
    __syncthreads();
    if (tid < NB) *(u32*)(lds + (tid * CAP + CAP - 1) * 8) = cnt[tid];
    __syncthreads();
    char* reg = ws + WS_R + (size_t)c * CAP * 8;
    for (int i = tid; i < NB * CAP / 2; i += 512) {
      const int b = i / (CAP / 2), o = i - b * (CAP / 2);
      *(u32x4*)(reg + (size_t)b * NCH * CAP * 8 + o * 16) = ((const u32x4*)lds)[i];
    }
    return;
  }
  const int n0 = (blockIdx.x - NCH) * 128;
#pragma unroll
  for (int i = 0; i < 4; ++i) {
    const int row = (w * 4 + i) * 4 + (l >> 4);
    const int node = min(n0 + row, NN - 1);
    const char* g = (const char*)x + node * 256 + (((l & 15) ^ (row & 15)) << 4);
    __builtin_amdgcn_global_load_lds((const u32*)g, (u32*)(lds + (w * 4 + i) * 1024), 16, 0, 0);
  }
  const int wq = w & 3, wt = w >> 2, c = l & 31, hf = l >> 5;
  const int cb = (wq & 1) * 128 + 4 * c, isq = wq >> 1;
  u32x4 bfr[4][8];
  {
    const u16* wp = w1 + (isq * 128 + 8 * hf) * 256 + cb;
    u32x2 raw[8][8];
#pragma unroll
    for (int ks = 0; ks < 8; ++ks)
#pragma unroll
      for (int j = 0; j < 8; ++j) raw[ks][j] = *(const u32x2*)(wp + (16 * ks + j) * 256);
    __builtin_amdgcn_sched_barrier(0);
#pragma unroll
    for (int ks = 0; ks < 8; ++ks) {
#pragma unroll
      for (int jp = 0; jp < 4; ++jp) {
        const u32x2 r0 = raw[ks][2 * jp], r1 = raw[ks][2 * jp + 1];
        bfr[0][ks][jp] = __builtin_amdgcn_perm(r1[0], r0[0], 0x05040100u);
        bfr[1][ks][jp] = __builtin_amdgcn_perm(r1[0], r0[0], 0x07060302u);
        bfr[2][ks][jp] = __builtin_amdgcn_perm(r1[1], r0[1], 0x05040100u);
        bfr[3][ks][jp] = __builtin_amdgcn_perm(r1[1], r0[1], 0x07060302u);
      }
    }
  }
  float bias[4];
  {
    const u32x2 bb = *(const u32x2*)(b1 + cb);
    bias[0] = isq ? bf2f(bb[0] & 0xFFFFu) * -LOG2E : 0.f;
    bias[1] = isq ? bf2f(bb[0] >> 16) * -LOG2E : 0.f;
    bias[2] = isq ? bf2f(bb[1] & 0xFFFFu) * -LOG2E : 0.f;
    bias[3] = isq ? bf2f(bb[1] >> 16) * -LOG2E : 0.f;
  }
  asm volatile("s_waitcnt vmcnt(0)" ::: "memory");
  __syncthreads();
  char* tb = ws + WS_T + ((size_t)(cb >> 6) * 2 * NN + (size_t)isq * NN) * 128 + (cb & 63) * 2;
#pragma unroll 1
  for (int ti = 0; ti < 2; ++ti) {
    const int tile = wt + 2 * ti;
    if (n0 + tile * 32 >= NN) break;
    f32x16 acc[4];
#pragma unroll
    for (int t = 0; t < 4; ++t)
#pragma unroll
      for (int r = 0; r < 16; ++r) acc[t][r] = 0.f;
    const int row = tile * 32 + c;
#pragma unroll
    for (int ks = 0; ks < 8; ++ks) {
      const bf16x8 a = *(const bf16x8*)(lds + row * 256 + (((2 * ks + hf) ^ (row & 15)) << 4));
#pragma unroll
      for (int t = 0; t < 4; ++t) acc[t] = MFMA(a, __builtin_bit_cast(bf16x8, bfr[t][ks]), acc[t]);
    }
#pragma unroll
    for (int r = 0; r < 16; ++r) {
      const int node = n0 + tile * 32 + (r & 3) + 8 * (r >> 2) + 4 * hf;
      u32x2 o = {pk_bf16(fmaf(acc[0][r], -LOG2E, bias[0]), fmaf(acc[1][r], -LOG2E, bias[1])),
                 pk_bf16(fmaf(acc[2][r], -LOG2E, bias[2]), fmaf(acc[3][r], -LOG2E, bias[3]))};
      if (node < NN) *(u32x2*)(tb + (size_t)node * 128) = o;
    }
  }
}

constexpr int L_LIST = 0;
constexpr int L_DEG = (NPB + 1) * LST * 4 + 16 - ((NPB + 1) * LST * 4) % 16;
constexpr int L_ORD = L_DEG + 400 * 4;
constexpr int L_HIST = L_ORD + 800;
constexpr int L_MISC = L_HIST + 256;
constexpr int L_FAST = L_MISC + 16;
constexpr int L_SLOW = NPB * 64 * 4 + 400 * 4;
constexpr int L2_BYTES = (L_FAST > L_SLOW ? L_FAST : L_SLOW) + 16;

__global__ __launch_bounds__(1024, 4) void k2(const u16* __restrict__ ea, const int* __restrict__ src,
                                              const int* __restrict__ dst, const u16* __restrict__ w1,
                                              const u16* __restrict__ w2, char* __restrict__ ws) {
  extern __shared__ __attribute__((aligned(16))) char lds[];
  const int tid = threadIdx.x, l = tid & 63, w = __builtin_amdgcn_readfirstlane(tid >> 6);
  const int slice = (blockIdx.x & 7) >> 1, bucket = (blockIdx.x >> 3) * 2 + (blockIdx.x & 1);
  const int nb0 = bucket * NPB, nval = min(NPB, NN - nb0);
  u32* list = (u32*)(lds + L_LIST);
  u32* deg = (u32*)(lds + L_DEG);
  u16* order = (u16*)(lds + L_ORD);
  u32* hist = (u32*)(lds + L_HIST);
  u32* misc = (u32*)(lds + L_MISC);
  constexpr int NU = NCH * CAP / 2;
  const u32x4* reg = (const u32x4*)(ws + WS_R + (size_t)bucket * NCH * CAP * 8);
  u32x4 rec[5];
#pragma unroll
  for (int i = 0; i < 5; ++i) {
    const int u = tid + 1024 * i;
    rec[i] = u32x4{0u, 0xFFFFFFFFu, 0u, 0xFFFFFFFFu};
    if (u < NU) rec[i] = reg[u];
  }
  if (blockIdx.x < 64 && w == 15) {
    const int f = blockIdx.x, cp = f >> 5, t = (f >> 4) & 1, ks = f & 15;
    const u16* p = w2 + (16 * ks + 8 * (l >> 5)) * 128 + 64 * cp + 2 * (l & 31) + t;
    u32x4 o;
#pragma unroll
    for (int jp = 0; jp < 4; ++jp) o[jp] = (u32)p[2 * jp * 128] | ((u32)p[(2 * jp + 1) * 128] << 16);
    *(u32x4*)(ws + WS_W2 + f * 1024 + l * 16) = o;
  }
  {
    const u32x4 pad = {0xFFFF0000u, 0xFFFF0000u, 0xFFFF0000u, 0xFFFF0000u};
    for (int i = tid; i < L_DEG / 16; i += 1024) ((u32x4*)lds)[i] = pad;
    if (tid < 400) { deg[tid] = 0; order[tid] = NPB; }
    if (tid < 64) hist[tid] = 0;
    if (tid < 2) misc[tid] = tid == 0 ? 16 : 0;
  }
  __syncthreads();
  {
    bool bad = false;
#pragma unroll
    for (int i = 0; i < 5; ++i) {
#pragma unroll
      for (int j = 0; j < 2; ++j) {
        const u32 dl = rec[i][2 * j + 1], pl = rec[i][2 * j];
        if (dl != 0xFFFFFFFFu) {
          const u32 r = atomicAdd(&deg[dl], 1u);
          if (r < LST) list[dl * LST + r] = pl; else bad = true;
        } else if (pl > CAP - 1) bad = true;
      }
    }
    if (bad) misc[1] = 1;
  }
  __syncthreads();
  u32 myd = 0, myr = 0;
  if (tid < nval) { myd = min(deg[tid], 63u); myr = atomicAdd(&hist[myd], 1u); }
  __syncthreads();
  if (w < 7) {
    u32 s = hist[l];
    const u32 own = s;
#pragma unroll
    for (int o = 1; o < 64; o <<= 1) { const u32 v = __shfl_down(s, o); if (l + o < 64) s += v; }
    const u32 st = __shfl(s - own, myd);
    if (tid < nval) order[st + myr] = tid;
  }
  __syncthreads();
  const char* tsl = ws + WS_T + (size_t)slice * 2 * NN * 128;
  char* sg = ws + WS_S + slice * 128;
  bool redo = misc[1] != 0;
  if (!redo) {
    if (slice == 0 && tid < nval) ((float*)(ws + WS_DEG))[nb0 + tid] = (float)deg[tid];
    bf16x8 id0, id1, aw;
    {
      const int r = l & 31, h = l >> 5, hfr = (r >> 2) & 1, rg = (r & 3) + 4 * (r >> 3), ir = rg >> 3, jr = rg & 7;
#pragma unroll
      for (int j = 0; j < 8; ++j) {
        const short one = (hfr == h && jr == j) ? (short)0x3F80 : (short)0;
        id0[j] = ir == 0 ? one : (short)0;
        id1[j] = ir == 1 ? one : (short)0;
      }
#pragma unroll
      for (int q = 0; q < 4; ++q) {
        const float wv = bf2f(w1[256 * 256 + slice * 64 + 32 * ir + 8 * q + jr]) * -LOG2E;
        const u32 hi = pk_bf16(wv, 0.f) & 0xFFFFu;
        const u32 lo = pk_bf16(wv - bf2f(hi), 0.f) & 0xFFFFu;
        aw[q] = hfr == h ? (short)hi : (short)0;
        aw[4 + q] = hfr == h ? (short)lo : (short)0;
      }
    }
    const __amdgpu_buffer_rsrc_t rs = __builtin_amdgcn_make_buffer_rsrc((void*)tsl, 0, 2 * NN * 128, 0x00020000);
    const u32 lo16 = (l & 3) * 16;
    const int ngr = (nval + 15) >> 4;
    bool ovf = false;
    int g = w;
    while (g < ngr) {
      const u32 node = order[g * 16 + (l >> 2)];
      const int len = deg[node];
      const int L = __builtin_amdgcn_readfirstlane(len);
      const u32* lp = list + node * LST;
      f32x16 q, nn, dd;
      const u32 qo = (NN + nb0 + node) * 128 + lo16;
      u32x4 a0 = __builtin_amdgcn_raw_buffer_load_b128(rs, qo, 0, 0);
      u32x4 a1 = __builtin_amdgcn_raw_buffer_load_b128(rs, qo + 64, 0, 0);
      u32 en0 = lp[0], en1 = lp[1];
      u32x4 b0 = __builtin_amdgcn_raw_buffer_load_b128(rs, (en0 >> 16) * 128 + lo16, 0, 0);
      u32x4 b1 = __builtin_amdgcn_raw_buffer_load_b128(rs, (en0 >> 16) * 128 + lo16 + 64, 0, 0);
      u32x4 c0 = __builtin_amdgcn_raw_buffer_load_b128(rs, (en1 >> 16) * 128 + lo16, 0, 0);
      u32x4 c1 = __builtin_amdgcn_raw_buffer_load_b128(rs, (en1 >> 16) * 128 + lo16 + 64, 0, 0);
#pragma unroll
      for (int r = 0; r < 16; ++r) { q[r] = 0.f; nn[r] = 0.f; dd[r] = 1.f; }
      q = MFMA(id0, __builtin_bit_cast(bf16x8, a0), q);
      q = MFMA(id1, __builtin_bit_cast(bf16x8, a1), q);
      for (int k = 0; k < L; ++k) {
        const u32 en2 = lp[min(k + 2, LST - 1)];
        const u64 es = (u64)(en0 & 0xFFFFu) << lo16;
        const u32x4 be = {(u32)es, (u32)(es >> 32), (u32)es, (u32)(es >> 32)};
        f32x16 z = MFMA(aw, __builtin_bit_cast(bf16x8, be), q);
        z = MFMA(id0, __builtin_bit_cast(bf16x8, b0), z);
        z = MFMA(id1, __builtin_bit_cast(bf16x8, b1), z);
        b0 = c0; b1 = c1; en0 = en1; en1 = en2;
        c0 = __builtin_amdgcn_raw_buffer_load_b128(rs, (en2 >> 16) * 128 + lo16, 0, 0);
        c1 = __builtin_amdgcn_raw_buffer_load_b128(rs, (en2 >> 16) * 128 + lo16 + 64, 0, 0);
        if (k < len) {
#pragma unroll
          for (int r = 0; r < 16; r += 2) {
            const f32x2 t = {z[r], z[r + 1]};
            const f32x2 u = {__builtin_amdgcn_exp2f(z[r]), __builtin_amdgcn_exp2f(z[r + 1])};
            f32x2 n2 = {nn[r], nn[r + 1]}, d2 = {dd[r], dd[r + 1]};
            n2 = __builtin_elementwise_fma(n2, u, n2);
            n2 = __builtin_elementwise_fma(t, d2, n2);
            d2 = __builtin_elementwise_fma(d2, u, d2);
            nn[r] = n2[0]; nn[r + 1] = n2[1]; dd[r] = d2[0]; dd[r + 1] = d2[1];
          }
        }
      }
      float ds = 0.f, na = 0.f;
      u32x4 o0, o1;
#pragma unroll
      for (int r = 0; r < 16; r += 2) {
        ds += dd[r] + dd[r + 1];
        na += __builtin_fabsf(nn[r]) + __builtin_fabsf(nn[r + 1]);
        const u32 v = pk_bf16(nn[r] * __builtin_amdgcn_rcpf(dd[r]), nn[r + 1] * __builtin_amdgcn_rcpf(dd[r + 1]));
        if (r < 8) o0[r >> 1] = v; else o1[(r - 8) >> 1] = v;
      }
      if (!(ds < 1e36f) || !(na < 3e38f)) ovf = true;
      if (node < (u32)nval) {
        char* sp = sg + (size_t)(nb0 + node) * 512 + lo16;
        *(u32x4*)sp = o0;
        *(u32x4*)(sp + 64) = o1;
      }
      u32 nx = 0;
      if (l == 0) nx = atomicAdd(&misc[0], 1u);
      g = __builtin_amdgcn_readfirstlane(nx);
    }
    if (ovf) misc[1] = 1;
    asm volatile("s_waitcnt vmcnt(0)" ::: "memory");
    __syncthreads();
    redo = misc[1] != 0;
  }
  if (redo) {
    __syncthreads();
    float* sum = (float*)lds;
    u32* dg = (u32*)(lds + NPB * 64 * 4);
    for (int i = tid; i < NPB * 64; i += 1024) sum[i] = 0.f;
    if (tid < 400) dg[tid] = 0;
    __syncthreads();
    const float wv = bf2f(w1[256 * 256 + slice * 64 + l]) * -LOG2E;
    const u16* tp = (const u16*)tsl;
    for (int e0 = w * 64; e0 < NE; e0 += 1024) {
      const u32 dl = (u32)(dst[e0 + l] - nb0);
      u64 m = __ballot(dl < (u32)nval);
      while (m) {
        const int j = __ffsll((long long)m) - 1;
        m &= m - 1;
        const u32 d = __builtin_amdgcn_readlane(dl, j);
        const int s = src[e0 + j];
        const float t = bf2f(tp[s * 64 + l]) + bf2f(tp[(NN + nb0 + d) * 64 + l]) + bf2f(ea[e0 + j]) * wv;
        atomicAdd(&sum[d * 64 + l], t / (1.f + __builtin_amdgcn_exp2f(t)));
        if (l == 0) atomicAdd(&dg[d], 1u);
      }
    }
    __syncthreads();
    for (int i = tid; i < nval * 32; i += 1024) {
      const int n = i >> 5, c = i & 31;
      *(u32*)(sg + (size_t)(nb0 + n) * 512 + c * 4) = pk_bf16(sum[n * 64 + 2 * c], sum[n * 64 + 2 * c + 1]);
    }
    if (slice == 0 && tid < nval) ((float*)(ws + WS_DEG))[nb0 + tid] = (float)dg[tid];
  }
}

__global__ __launch_bounds__(512, 2) void k3(const u16* __restrict__ b2, const char* __restrict__ ws,
                                             u16* __restrict__ out) {
  extern __shared__ __attribute__((aligned(16))) char lds[];
  const int tid = threadIdx.x, l = tid & 63, w = __builtin_amdgcn_readfirstlane(tid >> 6);
  const int n0 = blockIdx.x * 128;
#pragma unroll
  for (int i = 0; i < 8; ++i) {
    const int row = (w * 8 + i) * 2 + (l >> 5);
    const int node = min(n0 + row, NN - 1);
    const char* g = ws + WS_S + (size_t)node * 512 + (((l & 31) ^ (row & 15)) << 4);
    __builtin_amdgcn_global_load_lds((const u32*)g, (u32*)(lds + (w * 8 + i) * 1024), 16, 0, 0);
  }
  const int cp = w & 1, tile = w >> 1, c = l & 31, hf = l >> 5;
  u32x4 bfr[2][16];
#pragma unroll
  for (int t = 0; t < 2; ++t)
#pragma unroll
    for (int ks = 0; ks < 16; ++ks) bfr[t][ks] = *(const u32x4*)(ws + WS_W2 + ((cp * 2 + t) * 16 + ks) * 1024 + l * 16);
  float dg[16];
#pragma unroll
  for (int r = 0; r < 16; ++r)
    dg[r] = ((const float*)(ws + WS_DEG))[min(n0 + tile * 32 + (r & 3) + 8 * (r >> 2) + 4 * hf, NN - 1)];
  const u32 bb = *(const u32*)(b2 + 64 * cp + 2 * c);
  const float bias0 = bf2f(bb & 0xFFFFu), bias1 = bf2f(bb >> 16);
  asm volatile("s_waitcnt vmcnt(0)" ::: "memory");
  __syncthreads();
  if (n0 + tile * 32 >= NN) return;
  f32x16 acc[2];
#pragma unroll
  for (int r = 0; r < 16; ++r) { acc[0][r] = 0.f; acc[1][r] = 0.f; }
  const int row = tile * 32 + c;
#pragma unroll
  for (int ks = 0; ks < 16; ++ks) {
    const bf16x8 a = *(const bf16x8*)(lds + row * 512 + (((2 * ks + hf) ^ (row & 15)) << 4));
    acc[0] = MFMA(a, __builtin_bit_cast(bf16x8, bfr[0][ks]), acc[0]);
    acc[1] = MFMA(a, __builtin_bit_cast(bf16x8, bfr[1][ks]), acc[1]);
  }
#pragma unroll
  for (int r = 0; r < 16; ++r) {
    const int node = n0 + tile * 32 + (r & 3) + 8 * (r >> 2) + 4 * hf;
    const u32 o = pk_bf16(acc[0][r] * -LN2 + dg[r] * bias0, acc[1][r] * -LN2 + dg[r] * bias1);
    if (node < NN) *(u32*)(out + (size_t)node * 128 + 64 * cp + 2 * c) = o;
  }
}

extern "C" void kernel_launch(void* const* d_in, const int* in_sizes, int n_in, void* d_out, int out_size,
                              void* d_ws, size_t ws_size, hipStream_t stream) {
  const u16* x = (const u16*)d_in[0];
  const u16* ea = (const u16*)d_in[1];
  const int* src = (const int*)d_in[2];
  const int* dst = (const int*)d_in[3];
  const u16* w1 = (const u16*)d_in[4];
  const u16* b1 = (const u16*)d_in[5];
  const u16* w2 = (const u16*)d_in[6];
  const u16* b2 = (const u16*)d_in[7];
  char* ws = (char*)d_ws;
  constexpr int l1 = NB * CAP * 8 + 256, l3 = 65536;
  (void)hipFuncSetAttribute((const void*)k1, hipFuncAttributeMaxDynamicSharedMemorySize, l1);
  (void)hipFuncSetAttribute((const void*)k2, hipFuncAttributeMaxDynamicSharedMemorySize, L2_BYTES);
  (void)hipFuncSetAttribute((const void*)k3, hipFuncAttributeMaxDynamicSharedMemorySize, l3);
  k1<<<NCH + NG1, 512, l1, stream>>>(x, ea, src, dst, w1, b1, ws);
  k2<<<256, 1024, L2_BYTES, stream>>>(ea, src, dst, w1, w2, ws);
  k3<<<NG1, 512, l3, stream>>>(b2, ws, (u16*)d_out);
}
```

```cpp
#include <hip/hip_runtime.h>
#include <hip/hip_bf16.h>


typedef unsigned int u32;
typedef unsigned short u16;
typedef unsigned long long u64;
using bf16x8 = __attribute__((ext_vector_type(8))) short;
using s16x4 = __attribute__((ext_vector_type(4))) short;
using f32x16 = __attribute__((ext_vector_type(16))) float;
using f32x4 = __attribute__((ext_vector_type(4))) float;
using f32x2 = __attribute__((ext_vector_type(2))) float;
using u32x4 = __attribute__((ext_vector_type(4))) u32;
using u32x2 = __attribute__((ext_vector_type(2))) u32;
using bf16x2 = __attribute__((ext_vector_type(2))) __bf16;
#define LAS __attribute__((address_space(3)))

#define DI __device__ __forceinline__
#define MFMA(a, b, c) __builtin_amdgcn_mfma_f32_32x32x16_bf16((a), (b), (c), 0, 0, 0)

constexpr int NN = 25000, NE = 400000;
constexpr int NB = 64, NPB = 391;
constexpr int EPC = 1564;
constexpr int NPW = 192;
constexpr int LST = 44;
constexpr int SC1 = 16;
constexpr u32 OOB = 0x7FFFFF00u;
constexpr float LOG2E = 1.4426950408889634f, LN2 = 0.6931471805599453f;

#define CELL(bk, ck) ((ck) * (NB + 3) + (bk))
constexpr int NCELL = 17408;
constexpr size_t WS_T = 0;
constexpr size_t WS_S = WS_T + (size_t)4 * 2 * NN * 128;
constexpr size_t WS_R = WS_S + (size_t)NB * 25 * 8192;
constexpr size_t WS_R1 = WS_R + (size_t)NCELL * 128;
constexpr size_t WS_O = WS_R1 + (size_t)NCELL * 256;
constexpr size_t WS_F = WS_O + 256 * NB * 4;

extern "C" __device__ float mul1(float, float) __asm("llvm.amdgcn.fmul.legacy");
DI float bf2f(u32 h) { return __uint_as_float(h << 16); }
DI u32 pk_bf16(float a, float b) {
  f32x2 v = {a, b};
  return __builtin_bit_cast(u32, __builtin_convertvector(v, bf16x2));
}
DI u32x4 make_srd(const void* p, u32 stride, u32 n) {
  const u64 a = (u64)p;
  return u32x4{(u32)__builtin_amdgcn_readfirstlane((u32)a), (u32)__builtin_amdgcn_readfirstlane((u32)(a >> 32)) | (stride << 16),
               (u32)__builtin_amdgcn_readfirstlane(n), 0x00020000u};
}
DI __amdgpu_buffer_rsrc_t make_rsrc(const void* p, u32 n) { return __builtin_amdgcn_make_buffer_rsrc((void*)p, 0, n, 0x00020000); }
DI void dma16(u32 voff, u32x4 srd, u32 soff, u32 ldst) {
  u32 keep;
  asm volatile("s_mov_b32 %0, m0\n\ts_mov_b32 m0, %4\n\ts_nop 0\n\tbuffer_load_dwordx4 %1, %2, %3 offen lds\n\ts_mov_b32 m0, %0" : "=&s"(keep) : "v"(voff), "s"(srd), "s"(soff), "s"(ldst) : "memory");
}
DI void touch(u32 voff, u32x4 srd, u32 ldst) {
  u32 keep;
  asm volatile("s_mov_b32 %0, m0\n\ts_mov_b32 m0, %3\n\ts_nop 0\n\tbuffer_load_dword %1, %2, 0 offen lds\n\ts_mov_b32 m0, %0" : "=&s"(keep) : "v"(voff), "s"(srd), "s"(ldst) : "memory");
}
DI void lds_barrier() {
  __builtin_amdgcn_sched_barrier(0);
  asm volatile("s_waitcnt lgkmcnt(0)\n\ts_barrier" ::: "memory");
  __builtin_amdgcn_sched_barrier(0);
}
DI u32 scan64(u32 s) {
  s += __builtin_amdgcn_update_dpp(0u, s, 0x111, 0xF, 0xF, false);
  s += __builtin_amdgcn_update_dpp(0u, s, 0x112, 0xF, 0xF, false);
  s += __builtin_amdgcn_update_dpp(0u, s, 0x114, 0xF, 0xF, false);
  s += __builtin_amdgcn_update_dpp(0u, s, 0x118, 0xF, 0xF, false);
  s += __builtin_amdgcn_update_dpp(0u, s, 0x142, 0xA, 0xF, false);
  s += __builtin_amdgcn_update_dpp(0u, s, 0x143, 0xC, 0xF, false);
  return s;
}
DI bf16x8 tr_frag(u32 a0, u32 a1) {
  const s16x4 lo = __builtin_amdgcn_ds_read_tr16_b64_v4i16((LAS s16x4*)(size_t)a0);
  const s16x4 hi = __builtin_amdgcn_ds_read_tr16_b64_v4i16((LAS s16x4*)(size_t)a1);
  return __builtin_shufflevector(lo, hi, 0, 1, 2, 3, 4, 5, 6, 7);
}

constexpr int K1_W = 0, K1_X = 65536, K1_S = K1_X + 56 * 1040, K1_C = K1_S + 32768, K1_R = 0,
              K1_B = K1_C + 528, K1_BYTES = K1_B + 1024;

DI u32x4 repack(u32x4 v, int n, int l) {
  u32x4 o;
  o[3] = 0;
#pragma unroll
  for (int j = 0; j < 3; ++j) {
    o[j] = v[j] & 0x7FFFFFFFu;
    o[3] |= (n > j ? ((v[3] >> (8 * j)) & 0xFFu) | ((v[j] >> 31) << 8) : 400u + l) << (10 * j);
  }
  return o;
}

extern "C" __global__ __launch_bounds__(768, 3) void k1(const u16* __restrict__ x, const u16* __restrict__ ea,
                                             const int* __restrict__ src, const int* __restrict__ dst,
                                             const u16* __restrict__ w1, const u16* __restrict__ b1,
                                             char* __restrict__ ws) {
  extern __shared__ __attribute__((aligned(16))) char lds[];
  const int tid = threadIdx.x, l = tid & 63, w = __builtin_amdgcn_readfirstlane(tid >> 6);
  const int b = blockIdx.x;
  const bool lng = (b >> 4) * 8 + (b & 7) < 14;
  if (w >= 8) {
    const int t = tid - 512;
    {
      u64 pc;
      u32 off, len, d0, d1, d2;
      asm volatile("s_getpc_b64 %0\nmpnn_pc:\n\ts_mov_b32 %1, mpnn_pc-k1\n\ts_mov_b32 %2, mpnn_end-k1" : "=s"(pc), "=s"(off), "=s"(len));
      pc -= off;
      const u32 o = ((b >> 3) * 4 + (w - 8)) * 64;
      asm volatile("s_load_dword %0, %3, %4\n\ts_load_dword %1, %3, %5\n\ts_load_dword %2, %3, %6\n\ts_waitcnt lgkmcnt(0)"
                   : "=&s"(d0), "=&s"(d1), "=&s"(d2) : "s"(pc), "s"(min(o, len)), "s"(min(o + 8192, len)), "s"(min(o + 16384, len)));
    }
    const __amdgpu_buffer_rsrc_t sd = make_rsrc(dst, NE * 4), ss = make_rsrc(src, NE * 4), se = make_rsrc(ea, NE * 2);
    u32* cnt = (u32*)(lds + K1_C);
    if (t < 132) cnt[t] = 0;
    if (!lng) __builtin_amdgcn_s_setprio(3);
    lds_barrier();
    u32x4 vd[2], vs[2];
    u32x2 ve[2];
    bool ev[2];
#pragma unroll
    for (int i = 0; i < 2; ++i) {
      const int e = b * EPC + 4 * (t + 256 * i);
      ev[i] = t + 256 * i < EPC / 4 && e < NE;
      vd[i] = __builtin_amdgcn_raw_buffer_load_b128(sd, ev[i] ? e * 4 : OOB, 0, 0);
    }
#pragma unroll
    for (int i = 0; i < 2; ++i) {
      const int e = b * EPC + 4 * (t + 256 * i);
      vs[i] = __builtin_amdgcn_raw_buffer_load_b128(ss, ev[i] ? e * 4 : OOB, 0, 0);
      ve[i] = __builtin_amdgcn_raw_buffer_load_b64(se, ev[i] ? e * 2 : OOB, 0, 0);
    }
    lds_barrier();
    u32 bk[8], rk[8];
#pragma unroll
    for (int j = 0; j < 8; ++j) {
      bk[j] = vd[j >> 2][j & 3] / NPB;
      rk[j] = atomicAdd(&cnt[ev[j >> 2] ? bk[j] : NB + l], 1u);
    }
#pragma unroll
    for (int j = 0; j < 8; ++j) {
      const u32 dl = vd[j >> 2][j & 3] - bk[j] * NPB, un = (rk[j] * 171u) >> 9, sl = rk[j] - 3 * un;
      const u32 a = ev[j >> 2] && rk[j] < 72 ? K1_R + bk[j] * 400 + un * 16 : K1_R + NB * 400 + l * 16;
      *(u32*)(lds + a + (sl & 3) * 4) = ((ve[j >> 2][(j >> 1) & 1] >> (16 * (j & 1))) & 0xFFFFu) | (vs[j >> 2][j & 3] << 16) | ((dl >> 8) << 31);
      *(unsigned char*)(lds + a + 12 + (sl & 3)) = (unsigned char)dl;
    }
    if (w == 10 && b < NB) __builtin_amdgcn_raw_buffer_store_b32(0u, make_rsrc(ws + WS_F, NB * 256), b * 256 + l * 4, 0, SC1);
    if (lng) {
      asm volatile("s_waitcnt lgkmcnt(0)" ::: "memory");
      if (l == 0) atomicAdd(&cnt[128], 1u);
      while (*(volatile LAS u32*)(size_t)(K1_C + 512) < 4u) __builtin_amdgcn_s_sleep(1);
      asm volatile("" ::: "memory");
    } else lds_barrier();
    const u32 own = cnt[l], ex = (max(min(own, 72u), 24u) - 22u) * 171u >> 9;
    u32 inc = ex;
    inc += __builtin_amdgcn_update_dpp(0u, inc, 0x111, 0xF, 0xF, false);
    inc += __builtin_amdgcn_update_dpp(0u, inc, 0x112, 0xF, 0xF, false);
    inc += __builtin_amdgcn_update_dpp(0u, inc, 0x114, 0xF, 0xF, false);
    inc += __builtin_amdgcn_update_dpp(0u, inc, 0x118, 0xF, 0xF, false);
    const u32 cw = own | ((inc - ex) << 16);
    if (w == 11) __builtin_amdgcn_raw_buffer_store_b32(cw, make_rsrc(ws + WS_O, 256 * NB * 4), (b * NB + l) * 4, 0, SC1);
    const __amdgpu_buffer_rsrc_t r0 = make_rsrc(ws + WS_R, NCELL * 128), r1 = make_rsrc(ws + WS_R1, NCELL * 256);
#pragma unroll
    for (int i = 0; i < 2; ++i) {
      const int cell = (t >> 3) + 32 * i;
      __builtin_amdgcn_raw_buffer_store_b128(repack(*(const u32x4*)(lds + K1_R + cell * 400 + (t & 7) * 16), (int)cnt[cell] - 3 * (t & 7), l), r0,
                                             CELL(cell, b) * 128 + (t & 7) * 16, 0, SC1);
    }
    const u32 mc = __builtin_amdgcn_ds_bpermute((t >> 2) * 4, cw);
    char* blk = lds + K1_R + 28672 + (w - 8) * 4096;
    for (int k = t & 3, n = (int)min(mc & 0xFFFFu, 72u) - 24 - 3 * k; __any(n > 0); k += 4, n -= 12)
      if (n > 0) *(u32x4*)(blk + ((mc >> 16) + k) * 16) = repack(*(const u32x4*)(lds + K1_R + (t >> 2) * 400 + 128 + k * 16), n, l);
    const int tot = (__builtin_amdgcn_readlane(inc, (w - 8) * 16 + 15) + 7) & ~7;
    for (int u = 0; u < tot; u += 64)
      __builtin_amdgcn_raw_buffer_store_b128(*(const u32x4*)(blk + (u + l) * 16), r1, u + l < tot ? CELL(0, b) * 256 + (w - 8) * 4096 + (u + l) * 16 : OOB, 0, SC1);
    return;
  }
  const int isq = (b >> 3) & 1, blk = (b >> 4) * 8 + (b & 7), n0 = blk * NPW, n1 = NN;
  const int nx = 128 * NPW + 32 * blk, nt = nx < NN ? 7 : 6;
  const int sl = w & 3, c = l & 31, hf = l >> 5;
  {
    const u32x4 sw = make_srd(w1, 0, 256 * 512), sx = make_srd(x, 0, n1 * 256), sb = make_srd(b1, 0, isq ? 512 : 0);
    const u32 vx = (l >> 4) * 256 + (((l & 15) ^ ((l >> 4) << 2)) << 4);
    const u32 vw0 = hf * 512 + ((c ^ (hf << 2)) << 4), vw1 = hf * 512 + ((c ^ ((2 + hf) << 2)) << 4);
    asm volatile("s_nop 4" ::: "memory");
    if (w == 1) dma16(l * 16, sb, 0, K1_B);
#pragma unroll
    for (int i = 0; i < 2; ++i) dma16(vx, sx, (n0 + 32 * i + 4 * w) * 256, K1_X + (8 * i + w) * 1040);
#pragma unroll
    for (int i = 0; i < 8; ++i) dma16((i & 1) ? vw1 : vw0, sw, (isq * 128 + 2 * (w * 8 + i)) * 512, K1_W + (w * 8 + i) * 1024);
#pragma unroll
    for (int i = 2; i < 4; ++i) dma16(vx, sx, (n0 + 32 * i + 4 * w) * 256, K1_X + (8 * i + w) * 1040);
    asm volatile("s_waitcnt vmcnt(0)\n\ts_barrier" ::: "memory");
#pragma unroll
    for (int i = 4; i < 6; ++i) dma16(vx, sx, (n0 + 32 * i + 4 * w) * 256, K1_X + (8 * i + w) * 1040);
    if (nt == 7) dma16(vx, sx, (nx + 4 * w) * 256, K1_X + (48 + w) * 1040);
  }
  bf16x8 af[2][8];
  {
    const int q = (l >> 2) & 3, r4 = 4 * ((l >> 4) & 1) + (l & 3), hc = 4 * (r4 & 1) + (r4 >> 1);
#pragma unroll
    for (int ks = 0; ks < 8; ++ks)
#pragma unroll
      for (int T = 0; T < 2; ++T) {
        const u32 a = K1_W + (8 * hf + q) * 512 + (((4 * (2 * sl + T) + (hc >> 1)) ^ (q << 2)) << 4) + (hc & 1) * 8;
        af[T][ks] = tr_frag(a + ks * 8192, a + ks * 8192 + 2048);
      }
  }
  f32x16 bias[2];
#pragma unroll
  for (int u = 0; u < 4; ++u) {
    const u32x4 bb = *(const u32x4*)(lds + K1_B + sl * 128 + (u >> 1) * 64 + hf * 32 + (u & 1) * 16);
#pragma unroll
    for (int j = 0; j < 4; ++j) {
      bias[u >> 1][8 * (u & 1) + 2 * j] = bf2f(bb[j] & 0xFFFFu);
      bias[u >> 1][8 * (u & 1) + 2 * j + 1] = __uint_as_float(bb[j] & 0xFFFF0000u);
    }
  }
  const float sc = isq ? -LOG2E : LOG2E;
  const u32x4 rt = make_srd(ws + WS_T + ((size_t)sl * 2 * NN + (size_t)isq * NN) * 128, 0, n1 * 128);
  char* stg = lds + K1_S + w * 4096;
#pragma unroll 1
  for (int tile = w >> 2; tile < nt; tile += 2) {
    if (tile >= 2 && nt == 6) __builtin_amdgcn_s_sleep(16);
    if (tile < 2 && isq && nt == 6) __builtin_amdgcn_s_sleep(22);
    const char* xr = lds + K1_X + (tile * 8 + (c >> 2)) * 1040 + (c & 3) * 256;
    bf16x8 xb[8];
#pragma unroll
    for (int ks = 0; ks < 8; ++ks) xb[ks] = *(const bf16x8*)(xr + (((2 * ks + hf) ^ ((c & 3) << 2)) << 4));
    f32x16 a0 = bias[0], a1 = bias[1];
#pragma unroll
    for (int ks = 0; ks < 8; ++ks) {
      a0 = MFMA(af[0][ks], xb[ks], a0);
      a1 = MFMA(af[1][ks], xb[ks], a1);
    }
    __builtin_amdgcn_sched_group_barrier(0x100, 4, 0);
#pragma unroll
    for (int ks = 0; ks < 4; ++ks) {
      __builtin_amdgcn_sched_group_barrier(0x8, 2, 0);
      __builtin_amdgcn_sched_group_barrier(0x100, 1, 0);
    }
    __builtin_amdgcn_sched_group_barrier(0x8, 8, 0);
#pragma unroll
    for (int u = 0; u < 4; ++u) {
      const f32x16& a = u < 2 ? a0 : a1;
      u32x4 o;
#pragma unroll
      for (int j = 0; j < 4; ++j)
        o[j] = pk_bf16(mul1(a[8 * (u & 1) + 2 * j], sc), mul1(a[8 * (u & 1) + 2 * j + 1], sc));
      *(u32x4*)(stg + c * 128 + (((4 * (u >> 1) + 2 * hf + (u & 1)) ^ (c & 7)) << 4)) = o;
    }
    const u32x4* rb = (const u32x4*)(stg + (l >> 3) * 128 + (((l & 7) ^ (l >> 3)) << 4));
    asm volatile("buffer_store_dwordx4 %0, %4, %5, %6 offen sc1\n\tbuffer_store_dwordx4 %1, %4, %5, %6 offen offset:1024 sc1\n\t"
                 "buffer_store_dwordx4 %2, %4, %5, %6 offen offset:2048 sc1\n\tbuffer_store_dwordx4 %3, %4, %5, %6 offen offset:3072 sc1\n\ts_nop 1"
                 :: "v"(rb[0]), "v"(rb[64]), "v"(rb[128]), "v"(rb[192]), "v"(l * 16), "s"(rt), "s"((tile < 6 ? n0 + tile * 32 : nx) * 128) : "memory");
    if (lng && tile < 2) asm volatile("s_waitcnt vmcnt(4)" ::: "memory");
    asm volatile("s_waitcnt vmcnt(8)" ::: "memory");
    if (tile < (lng ? 2 : 4)) asm volatile("s_barrier" ::: "memory");
  }
}

constexpr int L_DEG = 0;
constexpr int L_ORD = L_DEG + 464 * 4;
constexpr int L_WC = L_ORD + 3328;
constexpr int L_DUM = L_WC + 1024;
constexpr int L_AW = L_DUM + 256;
constexpr int L_CN = L_AW + 512;
constexpr int L_MISC = L_CN + 1024;
constexpr int L_LIST = 8192;
constexpr int L_W2 = L_LIST + 69632;
constexpr int L_OWN = L_W2 + 65536;
constexpr int L2_BYTES = L_OWN + 12288;
static_assert(L_MISC + 16 <= L_LIST && (NPB + 1) * LST * 4 + 16 <= 69632 && L2_BYTES <= 163840, "LDS map");

#define K2_MAIN_ASM \
  "s_nop 4\n" \
  "v_mov_b32 v113, 0\n" \
  "v_mov_b32 v115, 0\n" \
  "s_mov_b32 s40, %[g0]\n" \
  "s_mov_b32 s43, %[g1]\n" \
  "s_setprio 3\n" \
  "Lgrp%=:\n" \
  "v_lshl_add_u32 v122, s40, 7, v121\n" \
  "ds_read_b64 v[122:123], v122\n" \
  "s_waitcnt lgkmcnt(0)\n" \
  "v_readfirstlane_b32 s41, v122\n" \
  "v_and_b32 v111, 0xfffff, v122\n" \
  "v_add_u32 v118, %[nn], v123\n" \
  "buffer_load_dwordx4 v[80:83], v[118:119], %[rt], 0 idxen offen\n" \
  "buffer_load_dwordx4 v[84:87], v[118:119], %[rt], 0 idxen offen offset:64\n" \
  "v_lshrrev_b32 v116, 18, v122\n" \
  "s_lshr_b32 s41, s41, 20\n" \
  "s_max_u32 s41, s41, 4\n" \
  "s_lshl_b32 s42, s41, 2\n" \
  "v_add_u32 v116, v116, v125\n" \
  "v_lshlrev_b32 v117, 16, v118\n" \
  "Lpad%=:\n" \
  "v_cmp_gt_u32 vcc, s42, v116\n" \
  "s_and_b64 exec, exec, vcc\n" \
  "s_cbranch_scc0 Lpd%=\n" \
  "v_add_u32 v122, v111, v116\n" \
  "v_add_u32 v116, 16, v116\n" \
  "ds_write_b32 v122, v117\n" \
  "s_branch Lpad%=\n" \
  "Lpd%=:\n" \
  "s_mov_b64 exec, -1\n" \
  "ds_read_u16 v122, v111 offset:2\n" \
  "ds_read_u16 v123, v111 offset:6\n" \
  "s_cmp_eq_u32 s43, 31\n" \
  "s_cselect_b32 s42, 5, 0\n" \
  "s_bitcmp1_b32 s41, 0\n" \
  "s_cbranch_scc1 Lodd%=\n" \
  "ds_read_u16 v114, v111\n" \
  "ds_read_u16 v112, v111 offset:4\n" \
  "s_waitcnt lgkmcnt(2)\n" \
  "v_mov_b32 v118, v122\n" \
  "buffer_load_dwordx4 v[96:99], v[118:119], %[rt], 0 idxen offen\n" \
  "buffer_load_dwordx4 v[100:103], v[118:119], %[rt], 0 idxen offen offset:64\n" \
  "v_mov_b32 v118, v123\n" \
  "buffer_load_dwordx4 v[88:91], v[118:119], %[rt], 0 idxen offen\n" \
  "buffer_load_dwordx4 v[92:95], v[118:119], %[rt], 0 idxen offen offset:64\n" \
  "ds_read_u16 v118, v111 offset:10\n" \
  "s_waitcnt vmcnt(4)\n" \
  "s_cmp_eq_u32 s40, %[g0]\n" \
  "s_cbranch_scc1 Lsg1%=\n" \
  "s_cmp_lg_u32 %[bad], 0\n" \
  "s_cbranch_scc1 Lsg1%=\n" \
  "s_mov_b64 exec, 1\n" \
  "v_mov_b32 v122, 1\n" \
  "global_atomic_add v113, v122, %[fl] offset:128\n" \
  "s_mov_b64 exec, -1\n" \
  "Lsg1%=:\n" \
  "v_lshlrev_b32 v32, 16, v80\n" \
  "v_and_b32 v33, 0xffff0000, v80\n" \
  "v_lshlrev_b32 v34, 16, v81\n" \
  "v_and_b32 v35, 0xffff0000, v81\n" \
  "v_lshlrev_b32 v36, 16, v82\n" \
  "v_and_b32 v37, 0xffff0000, v82\n" \
  "v_lshlrev_b32 v38, 16, v83\n" \
  "v_and_b32 v39, 0xffff0000, v83\n" \
  "v_lshlrev_b32 v40, 16, v84\n" \
  "v_and_b32 v41, 0xffff0000, v84\n" \
  "v_lshlrev_b32 v42, 16, v85\n" \
  "v_and_b32 v43, 0xffff0000, v85\n" \
  "v_lshlrev_b32 v44, 16, v86\n" \
  "v_and_b32 v45, 0xffff0000, v86\n" \
  "v_lshlrev_b32 v46, 16, v87\n" \
  "v_and_b32 v47, 0xffff0000, v87\n" \
  "s_waitcnt lgkmcnt(0)\n" \
  "v_lshlrev_b64 v[116:117], v119, v[114:115]\n" \
  "s_nop 1\n" \
  "v_mfma_f32_32x32x8_bf16 v[0:15], v[108:109], v[116:117], v[32:47]\n" \
  "s_waitcnt vmcnt(2)\n" \
  "v_smfmac_f32_32x32x32_bf16 v[0:15], v[104:107], v[96:103], v110\n" \
  "buffer_load_dwordx4 v[96:99], v[118:119], %[rt], 0 idxen offen\n" \
  "buffer_load_dwordx4 v[100:103], v[118:119], %[rt], 0 idxen offen offset:64\n" \
  "ds_read_u16 v114, v111 offset:8\n" \
  "ds_read_u16 v118, v111 offset:14\n" \
  "v_lshlrev_b64 v[116:117], v119, v[112:113]\n" \
  "s_nop 7\n" \
  "v_mfma_f32_32x32x8_bf16 v[48:63], v[108:109], v[116:117], v[32:47]\n" \
  "v_exp_f32 v16, v0\n" \
  "v_exp_f32 v17, v1\n" \
  "v_exp_f32 v18, v2\n" \
  "s_waitcnt vmcnt(2)\n" \
  "v_smfmac_f32_32x32x32_bf16 v[48:63], v[104:107], v[88:95], v110\n" \
  "v_exp_f32 v19, v3\n" \
  "v_exp_f32 v20, v4\n" \
  "s_waitcnt lgkmcnt(0)\n" \
  "buffer_load_dwordx4 v[88:91], v[118:119], %[rt], 0 idxen offen\n" \
  "buffer_load_dwordx4 v[92:95], v[118:119], %[rt], 0 idxen offen offset:64\n" \
  "ds_read_u16 v112, v111 offset:12\n" \
  "ds_read_u16 v118, v111 offset:18\n" \
  "v_exp_f32 v21, v5\n" \
  "v_exp_f32 v22, v6\n" \
  "v_exp_f32 v23, v7\n" \
  "v_exp_f32 v24, v8\n" \
  "v_exp_f32 v25, v9\n" \
  "v_exp_f32 v26, v10\n" \
  "v_exp_f32 v27, v11\n" \
  "v_exp_f32 v28, v12\n" \
  "v_exp_f32 v29, v13\n" \
  "v_exp_f32 v30, v14\n" \
  "v_exp_f32 v31, v15\n" \
  "v_lshlrev_b64 v[116:117], v119, v[114:115]\n" \
  "v_pk_add_f32 v[16:17], v[16:17], 1.0 op_sel_hi:[1,0]\n" \
  "v_pk_add_f32 v[18:19], v[18:19], 1.0 op_sel_hi:[1,0]\n" \
  "v_pk_add_f32 v[20:21], v[20:21], 1.0 op_sel_hi:[1,0]\n" \
  "v_pk_add_f32 v[22:23], v[22:23], 1.0 op_sel_hi:[1,0]\n" \
  "v_pk_add_f32 v[24:25], v[24:25], 1.0 op_sel_hi:[1,0]\n" \
  "v_pk_add_f32 v[26:27], v[26:27], 1.0 op_sel_hi:[1,0]\n" \
  "v_pk_add_f32 v[28:29], v[28:29], 1.0 op_sel_hi:[1,0]\n" \
  "v_pk_add_f32 v[30:31], v[30:31], 1.0 op_sel_hi:[1,0]\n" \
  "v_add_u32 v111, 16, v111\n" \
  "s_sub_u32 s41, s41, 4\n" \
  "s_lshr_b32 s41, s41, 1\n" \
  "s_cmp_eq_u32 s41, 0\n" \
  "s_cbranch_scc1 Ltail%=\n" \
  "s_branch Ltop%=\n" \
  "Lodd%=:\n" \
  "ds_read_u16 v112, v111\n" \
  "ds_read_u16 v114, v111 offset:4\n" \
  "s_waitcnt lgkmcnt(2)\n" \
  "v_mov_b32 v118, v122\n" \
  "buffer_load_dwordx4 v[88:91], v[118:119], %[rt], 0 idxen offen\n" \
  "buffer_load_dwordx4 v[92:95], v[118:119], %[rt], 0 idxen offen offset:64\n" \
  "v_mov_b32 v118, v123\n" \
  "buffer_load_dwordx4 v[96:99], v[118:119], %[rt], 0 idxen offen\n" \
  "buffer_load_dwordx4 v[100:103], v[118:119], %[rt], 0 idxen offen offset:64\n" \
  "ds_read_u16 v118, v111 offset:10\n" \
  "s_waitcnt vmcnt(4)\n" \
  "s_cmp_eq_u32 s40, %[g0]\n" \
  "s_cbranch_scc1 Lsg0%=\n" \
  "s_cmp_lg_u32 %[bad], 0\n" \
  "s_cbranch_scc1 Lsg0%=\n" \
  "s_mov_b64 exec, 1\n" \
  "v_mov_b32 v122, 1\n" \
  "global_atomic_add v113, v122, %[fl] offset:128\n" \
  "s_mov_b64 exec, -1\n" \
  "Lsg0%=:\n" \
  "v_lshlrev_b32 v32, 16, v80\n" \
  "v_and_b32 v33, 0xffff0000, v80\n" \
  "v_lshlrev_b32 v34, 16, v81\n" \
  "v_and_b32 v35, 0xffff0000, v81\n" \
  "v_lshlrev_b32 v36, 16, v82\n" \
  "v_and_b32 v37, 0xffff0000, v82\n" \
  "v_lshlrev_b32 v38, 16, v83\n" \
  "v_and_b32 v39, 0xffff0000, v83\n" \
  "v_lshlrev_b32 v40, 16, v84\n" \
  "v_and_b32 v41, 0xffff0000, v84\n" \
  "v_lshlrev_b32 v42, 16, v85\n" \
  "v_and_b32 v43, 0xffff0000, v85\n" \
  "v_lshlrev_b32 v44, 16, v86\n" \
  "v_and_b32 v45, 0xffff0000, v86\n" \
  "v_lshlrev_b32 v46, 16, v87\n" \
  "v_and_b32 v47, 0xffff0000, v87\n" \
  "s_waitcnt lgkmcnt(0)\n" \
  "v_lshlrev_b64 v[116:117], v119, v[112:113]\n" \
  "s_nop 1\n" \
  "v_mfma_f32_32x32x8_bf16 v[0:15], v[108:109], v[116:117], v[32:47]\n" \
  "s_waitcnt vmcnt(2)\n" \
  "v_smfmac_f32_32x32x32_bf16 v[0:15], v[104:107], v[88:95], v110\n" \
  "buffer_load_dwordx4 v[88:91], v[118:119], %[rt], 0 idxen offen\n" \
  "buffer_load_dwordx4 v[92:95], v[118:119], %[rt], 0 idxen offen offset:64\n" \
  "ds_read_u16 v112, v111 offset:8\n" \
  "ds_read_u16 v118, v111 offset:14\n" \
  "v_lshlrev_b64 v[116:117], v119, v[114:115]\n" \
  "s_nop 7\n" \
  "v_mfma_f32_32x32x8_bf16 v[64:79], v[108:109], v[116:117], v[32:47]\n" \
  "v_exp_f32 v16, v0\n" \
  "v_exp_f32 v17, v1\n" \
  "v_exp_f32 v18, v2\n" \
  "s_waitcnt vmcnt(2)\n" \
  "v_smfmac_f32_32x32x32_bf16 v[64:79], v[104:107], v[96:103], v110\n" \
  "v_exp_f32 v19, v3\n" \
  "v_exp_f32 v20, v4\n" \
  "s_waitcnt lgkmcnt(0)\n" \
  "buffer_load_dwordx4 v[96:99], v[118:119], %[rt], 0 idxen offen\n" \
  "buffer_load_dwordx4 v[100:103], v[118:119], %[rt], 0 idxen offen offset:64\n" \
  "ds_read_u16 v114, v111 offset:12\n" \
  "ds_read_u16 v118, v111 offset:18\n" \
  "v_exp_f32 v21, v5\n" \
  "v_exp_f32 v22, v6\n" \
  "v_exp_f32 v23, v7\n" \
  "v_exp_f32 v24, v8\n" \
  "v_exp_f32 v25, v9\n" \
  "v_exp_f32 v26, v10\n" \
  "v_exp_f32 v27, v11\n" \
  "v_exp_f32 v28, v12\n" \
  "v_exp_f32 v29, v13\n" \
  "v_exp_f32 v30, v14\n" \
  "v_exp_f32 v31, v15\n" \
  "v_lshlrev_b64 v[116:117], v119, v[112:113]\n" \
  "v_pk_add_f32 v[16:17], v[16:17], 1.0 op_sel_hi:[1,0]\n" \
  "v_pk_add_f32 v[18:19], v[18:19], 1.0 op_sel_hi:[1,0]\n" \
  "v_pk_add_f32 v[20:21], v[20:21], 1.0 op_sel_hi:[1,0]\n" \
  "v_pk_add_f32 v[22:23], v[22:23], 1.0 op_sel_hi:[1,0]\n" \
  "v_pk_add_f32 v[24:25], v[24:25], 1.0 op_sel_hi:[1,0]\n" \
  "v_pk_add_f32 v[26:27], v[26:27], 1.0 op_sel_hi:[1,0]\n" \
  "v_pk_add_f32 v[28:29], v[28:29], 1.0 op_sel_hi:[1,0]\n" \
  "v_pk_add_f32 v[30:31], v[30:31], 1.0 op_sel_hi:[1,0]\n" \
  "v_add_u32 v111, 12, v111\n" \
  "s_sub_u32 s41, s41, 3\n" \
  "s_lshr_b32 s41, s41, 1\n" \
  "s_branch Lmid%=\n" \
  ".p2align 4\n" \
  "Ltop%=:\n" \
  "v_exp_f32 v80, v48\n" \
  "v_mfma_f32_32x32x8_bf16 v[64:79], v[108:109], v[116:117], v[32:47]\n" \
  "v_exp_f32 v81, v49\n" \
  "v_exp_f32 v82, v50\n" \
  "v_exp_f32 v83, v51\n" \
  "v_exp_f32 v84, v52\n" \
  "v_exp_f32 v85, v53\n" \
  "v_exp_f32 v86, v54\n" \
  "v_exp_f32 v87, v55\n" \
  "v_pk_fma_f32 v[0:1], v[0:1], v[80:81], v[0:1]\n" \
  "v_pk_fma_f32 v[2:3], v[2:3], v[82:83], v[2:3]\n" \
  "v_pk_fma_f32 v[4:5], v[4:5], v[84:85], v[4:5]\n" \
  "v_pk_fma_f32 v[6:7], v[6:7], v[86:87], v[6:7]\n" \
  "v_pk_fma_f32 v[0:1], v[48:49], v[16:17], v[0:1]\n" \
  "v_pk_fma_f32 v[2:3], v[50:51], v[18:19], v[2:3]\n" \
  "v_pk_fma_f32 v[4:5], v[52:53], v[20:21], v[4:5]\n" \
  "v_pk_fma_f32 v[6:7], v[54:55], v[22:23], v[6:7]\n" \
  "v_pk_fma_f32 v[16:17], v[16:17], v[80:81], v[16:17]\n" \
  "v_pk_fma_f32 v[18:19], v[18:19], v[82:83], v[18:19]\n" \
  "v_pk_fma_f32 v[20:21], v[20:21], v[84:85], v[20:21]\n" \
  "v_pk_fma_f32 v[22:23], v[22:23], v[86:87], v[22:23]\n" \
  "v_exp_f32 v80, v56\n" \
  "s_waitcnt vmcnt(2)\n" \
  "v_smfmac_f32_32x32x32_bf16 v[64:79], v[104:107], v[96:103], v110\n" \
  "v_exp_f32 v81, v57\n" \
  "v_exp_f32 v82, v58\n" \
  "s_waitcnt lgkmcnt(0)\n" \
  "buffer_load_dwordx4 v[96:99], v[118:119], %[rt], 0 idxen offen\n" \
  "buffer_load_dwordx4 v[100:103], v[118:119], %[rt], 0 idxen offen offset:64\n" \
  "ds_read_u16 v114, v111 offset:0\n" \
  "ds_read_u16 v118, v111 offset:6\n" \
  "v_exp_f32 v83, v59\n" \
  "v_exp_f32 v84, v60\n" \
  "v_exp_f32 v85, v61\n" \
  "v_exp_f32 v86, v62\n" \
  "v_exp_f32 v87, v63\n" \
  "v_lshlrev_b64 v[116:117], v119, v[112:113]\n" \
  "v_pk_fma_f32 v[8:9], v[8:9], v[80:81], v[8:9]\n" \
  "v_pk_fma_f32 v[10:11], v[10:11], v[82:83], v[10:11]\n" \
  "v_pk_fma_f32 v[12:13], v[12:13], v[84:85], v[12:13]\n" \
  "v_pk_fma_f32 v[14:15], v[14:15], v[86:87], v[14:15]\n" \
  "v_pk_fma_f32 v[8:9], v[56:57], v[24:25], v[8:9]\n" \
  "v_pk_fma_f32 v[10:11], v[58:59], v[26:27], v[10:11]\n" \
  "v_pk_fma_f32 v[12:13], v[60:61], v[28:29], v[12:13]\n" \
  "v_pk_fma_f32 v[14:15], v[62:63], v[30:31], v[14:15]\n" \
  "v_pk_fma_f32 v[24:25], v[24:25], v[80:81], v[24:25]\n" \
  "v_pk_fma_f32 v[26:27], v[26:27], v[82:83], v[26:27]\n" \
  "v_pk_fma_f32 v[28:29], v[28:29], v[84:85], v[28:29]\n" \
  "v_pk_fma_f32 v[30:31], v[30:31], v[86:87], v[30:31]\n" \
  "Lmid%=:\n" \
  "v_exp_f32 v80, v64\n" \
  "v_mfma_f32_32x32x8_bf16 v[48:63], v[108:109], v[116:117], v[32:47]\n" \
  "v_exp_f32 v81, v65\n" \
  "v_exp_f32 v82, v66\n" \
  "v_exp_f32 v83, v67\n" \
  "v_exp_f32 v84, v68\n" \
  "v_exp_f32 v85, v69\n" \
  "v_exp_f32 v86, v70\n" \
  "v_exp_f32 v87, v71\n" \
  "v_pk_fma_f32 v[0:1], v[0:1], v[80:81], v[0:1]\n" \
  "v_pk_fma_f32 v[2:3], v[2:3], v[82:83], v[2:3]\n" \
  "v_pk_fma_f32 v[4:5], v[4:5], v[84:85], v[4:5]\n" \
  "v_pk_fma_f32 v[6:7], v[6:7], v[86:87], v[6:7]\n" \
  "v_pk_fma_f32 v[0:1], v[64:65], v[16:17], v[0:1]\n" \
  "v_pk_fma_f32 v[2:3], v[66:67], v[18:19], v[2:3]\n" \
  "v_pk_fma_f32 v[4:5], v[68:69], v[20:21], v[4:5]\n" \
  "v_pk_fma_f32 v[6:7], v[70:71], v[22:23], v[6:7]\n" \
  "v_pk_fma_f32 v[16:17], v[16:17], v[80:81], v[16:17]\n" \
  "v_pk_fma_f32 v[18:19], v[18:19], v[82:83], v[18:19]\n" \
  "v_pk_fma_f32 v[20:21], v[20:21], v[84:85], v[20:21]\n" \
  "v_pk_fma_f32 v[22:23], v[22:23], v[86:87], v[22:23]\n" \
  "v_exp_f32 v80, v72\n" \
  "s_waitcnt vmcnt(2)\n" \
  "v_smfmac_f32_32x32x32_bf16 v[48:63], v[104:107], v[88:95], v110\n" \
  "v_exp_f32 v81, v73\n" \
  "v_exp_f32 v82, v74\n" \
  "s_waitcnt lgkmcnt(0)\n" \
  "buffer_load_dwordx4 v[88:91], v[118:119], %[rt], 0 idxen offen\n" \
  "buffer_load_dwordx4 v[92:95], v[118:119], %[rt], 0 idxen offen offset:64\n" \
  "ds_read_u16 v112, v111 offset:4\n" \
  "ds_read_u16 v118, v111 offset:10\n" \
  "v_exp_f32 v83, v75\n" \
  "v_exp_f32 v84, v76\n" \
  "v_exp_f32 v85, v77\n" \
  "v_exp_f32 v86, v78\n" \
  "v_exp_f32 v87, v79\n" \
  "v_lshlrev_b64 v[116:117], v119, v[114:115]\n" \
  "v_pk_fma_f32 v[8:9], v[8:9], v[80:81], v[8:9]\n" \
  "v_pk_fma_f32 v[10:11], v[10:11], v[82:83], v[10:11]\n" \
  "v_pk_fma_f32 v[12:13], v[12:13], v[84:85], v[12:13]\n" \
  "v_pk_fma_f32 v[14:15], v[14:15], v[86:87], v[14:15]\n" \
  "v_pk_fma_f32 v[8:9], v[72:73], v[24:25], v[8:9]\n" \
  "v_pk_fma_f32 v[10:11], v[74:75], v[26:27], v[10:11]\n" \
  "v_pk_fma_f32 v[12:13], v[76:77], v[28:29], v[12:13]\n" \
  "v_pk_fma_f32 v[14:15], v[78:79], v[30:31], v[14:15]\n" \
  "v_pk_fma_f32 v[24:25], v[24:25], v[80:81], v[24:25]\n" \
  "v_pk_fma_f32 v[26:27], v[26:27], v[82:83], v[26:27]\n" \
  "v_pk_fma_f32 v[28:29], v[28:29], v[84:85], v[28:29]\n" \
  "v_pk_fma_f32 v[30:31], v[30:31], v[86:87], v[30:31]\n" \
  "v_add_u32 v111, 8, v111\n" \
  "s_sub_u32 s41, s41, 1\n" \
  "s_cmp_gt_u32 s41, s42\n" \
  "s_cbranch_scc1 Ltop%=\n" \
  "s_cmp_eq_u32 s41, 0\n" \
  "s_cbranch_scc1 Ltail%=\n" \
  "s_cmp_gt_u32 s41, 2\n" \
  "s_cbranch_scc1 Lp2%=\n" \
  "s_setprio 1\n" \
  "s_mov_b32 s42, 0\n" \
  "s_branch Ltop%=\n" \
  "Lp2%=:\n" \
  "s_setprio 2\n" \
  "s_mov_b32 s42, 2\n" \
  "s_branch Ltop%=\n" \
  "Ltail%=:\n" \
  "s_cmp_eq_u32 s43, 31\n" \
  "s_cbranch_scc0 Ltl2%=\n" \
  "s_setprio 0\n" \
  "Ltl2%=:\n" \
  "v_exp_f32 v80, v48\n" \
  "v_mfma_f32_32x32x8_bf16 v[64:79], v[108:109], v[116:117], v[32:47]\n" \
  "v_exp_f32 v81, v49\n" \
  "v_exp_f32 v82, v50\n" \
  "v_exp_f32 v83, v51\n" \
  "v_exp_f32 v84, v52\n" \
  "v_exp_f32 v85, v53\n" \
  "v_exp_f32 v86, v54\n" \
  "v_exp_f32 v87, v55\n" \
  "v_pk_fma_f32 v[0:1], v[0:1], v[80:81], v[0:1]\n" \
  "v_pk_fma_f32 v[2:3], v[2:3], v[82:83], v[2:3]\n" \
  "v_pk_fma_f32 v[4:5], v[4:5], v[84:85], v[4:5]\n" \
  "v_pk_fma_f32 v[6:7], v[6:7], v[86:87], v[6:7]\n" \
  "v_pk_fma_f32 v[0:1], v[48:49], v[16:17], v[0:1]\n" \
  "v_pk_fma_f32 v[2:3], v[50:51], v[18:19], v[2:3]\n" \
  "v_pk_fma_f32 v[4:5], v[52:53], v[20:21], v[4:5]\n" \
  "v_pk_fma_f32 v[6:7], v[54:55], v[22:23], v[6:7]\n" \
  "v_pk_fma_f32 v[16:17], v[16:17], v[80:81], v[16:17]\n" \
  "v_pk_fma_f32 v[18:19], v[18:19], v[82:83], v[18:19]\n" \
  "v_pk_fma_f32 v[20:21], v[20:21], v[84:85], v[20:21]\n" \
  "v_pk_fma_f32 v[22:23], v[22:23], v[86:87], v[22:23]\n" \
  "v_exp_f32 v80, v56\n" \
  "s_waitcnt vmcnt(2)\n" \
  "v_smfmac_f32_32x32x32_bf16 v[64:79], v[104:107], v[96:103], v110\n" \
  "v_exp_f32 v81, v57\n" \
  "v_exp_f32 v82, v58\n" \
  "v_exp_f32 v83, v59\n" \
  "v_exp_f32 v84, v60\n" \
  "v_exp_f32 v85, v61\n" \
  "v_exp_f32 v86, v62\n" \
  "v_exp_f32 v87, v63\n" \
  "s_waitcnt lgkmcnt(0)\n" \
  "v_lshlrev_b64 v[116:117], v119, v[112:113]\n" \
  "v_pk_fma_f32 v[8:9], v[8:9], v[80:81], v[8:9]\n" \
  "v_pk_fma_f32 v[10:11], v[10:11], v[82:83], v[10:11]\n" \
  "v_pk_fma_f32 v[12:13], v[12:13], v[84:85], v[12:13]\n" \
  "v_pk_fma_f32 v[14:15], v[14:15], v[86:87], v[14:15]\n" \
  "v_pk_fma_f32 v[8:9], v[56:57], v[24:25], v[8:9]\n" \
  "v_pk_fma_f32 v[10:11], v[58:59], v[26:27], v[10:11]\n" \
  "v_pk_fma_f32 v[12:13], v[60:61], v[28:29], v[12:13]\n" \
  "v_pk_fma_f32 v[14:15], v[62:63], v[30:31], v[14:15]\n" \
  "v_pk_fma_f32 v[24:25], v[24:25], v[80:81], v[24:25]\n" \
  "v_pk_fma_f32 v[26:27], v[26:27], v[82:83], v[26:27]\n" \
  "v_pk_fma_f32 v[28:29], v[28:29], v[84:85], v[28:29]\n" \
  "v_pk_fma_f32 v[30:31], v[30:31], v[86:87], v[30:31]\n" \
  "v_exp_f32 v80, v64\n" \
  "v_mfma_f32_32x32x8_bf16 v[48:63], v[108:109], v[116:117], v[32:47]\n" \
  "v_exp_f32 v81, v65\n" \
  "v_exp_f32 v82, v66\n" \
  "v_exp_f32 v83, v67\n" \
  "v_exp_f32 v84, v68\n" \
  "v_exp_f32 v85, v69\n" \
  "v_exp_f32 v86, v70\n" \
  "v_exp_f32 v87, v71\n" \
  "v_pk_fma_f32 v[0:1], v[0:1], v[80:81], v[0:1]\n" \
  "v_pk_fma_f32 v[2:3], v[2:3], v[82:83], v[2:3]\n" \
  "v_pk_fma_f32 v[4:5], v[4:5], v[84:85], v[4:5]\n" \
  "v_pk_fma_f32 v[6:7], v[6:7], v[86:87], v[6:7]\n" \
  "v_pk_fma_f32 v[0:1], v[64:65], v[16:17], v[0:1]\n" \
  "v_pk_fma_f32 v[2:3], v[66:67], v[18:19], v[2:3]\n" \
  "v_pk_fma_f32 v[4:5], v[68:69], v[20:21], v[4:5]\n" \
  "v_pk_fma_f32 v[6:7], v[70:71], v[22:23], v[6:7]\n" \
  "v_pk_fma_f32 v[16:17], v[16:17], v[80:81], v[16:17]\n" \
  "v_pk_fma_f32 v[18:19], v[18:19], v[82:83], v[18:19]\n" \
  "v_pk_fma_f32 v[20:21], v[20:21], v[84:85], v[20:21]\n" \
  "v_pk_fma_f32 v[22:23], v[22:23], v[86:87], v[22:23]\n" \
  "v_exp_f32 v80, v72\n" \
  "s_waitcnt vmcnt(0)\n" \
  "v_smfmac_f32_32x32x32_bf16 v[48:63], v[104:107], v[88:95], v110\n" \
  "v_exp_f32 v81, v73\n" \
  "v_exp_f32 v82, v74\n" \
  "v_exp_f32 v83, v75\n" \
  "v_exp_f32 v84, v76\n" \
  "v_exp_f32 v85, v77\n" \
  "v_exp_f32 v86, v78\n" \
  "v_exp_f32 v87, v79\n" \
  "s_nop 0\n" \
  "v_pk_fma_f32 v[8:9], v[8:9], v[80:81], v[8:9]\n" \
  "v_pk_fma_f32 v[10:11], v[10:11], v[82:83], v[10:11]\n" \
  "v_pk_fma_f32 v[12:13], v[12:13], v[84:85], v[12:13]\n" \
  "v_pk_fma_f32 v[14:15], v[14:15], v[86:87], v[14:15]\n" \
  "v_pk_fma_f32 v[8:9], v[72:73], v[24:25], v[8:9]\n" \
  "v_pk_fma_f32 v[10:11], v[74:75], v[26:27], v[10:11]\n" \
  "v_pk_fma_f32 v[12:13], v[76:77], v[28:29], v[12:13]\n" \
  "v_pk_fma_f32 v[14:15], v[78:79], v[30:31], v[14:15]\n" \
  "v_pk_fma_f32 v[24:25], v[24:25], v[80:81], v[24:25]\n" \
  "v_pk_fma_f32 v[26:27], v[26:27], v[82:83], v[26:27]\n" \
  "v_pk_fma_f32 v[28:29], v[28:29], v[84:85], v[28:29]\n" \
  "v_pk_fma_f32 v[30:31], v[30:31], v[86:87], v[30:31]\n" \
  "v_exp_f32 v80, v48\n" \
  "v_exp_f32 v81, v49\n" \
  "v_exp_f32 v82, v50\n" \
  "v_exp_f32 v83, v51\n" \
  "v_exp_f32 v84, v52\n" \
  "v_exp_f32 v85, v53\n" \
  "v_exp_f32 v86, v54\n" \
  "v_exp_f32 v87, v55\n" \
  "s_nop 0\n" \
  "v_pk_fma_f32 v[0:1], v[0:1], v[80:81], v[0:1]\n" \
  "v_pk_fma_f32 v[2:3], v[2:3], v[82:83], v[2:3]\n" \
  "v_pk_fma_f32 v[4:5], v[4:5], v[84:85], v[4:5]\n" \
  "v_pk_fma_f32 v[6:7], v[6:7], v[86:87], v[6:7]\n" \
  "v_pk_fma_f32 v[0:1], v[48:49], v[16:17], v[0:1]\n" \
  "v_pk_fma_f32 v[2:3], v[50:51], v[18:19], v[2:3]\n" \
  "v_pk_fma_f32 v[4:5], v[52:53], v[20:21], v[4:5]\n" \
  "v_pk_fma_f32 v[6:7], v[54:55], v[22:23], v[6:7]\n" \
  "v_pk_fma_f32 v[16:17], v[16:17], v[80:81], v[16:17]\n" \
  "v_pk_fma_f32 v[18:19], v[18:19], v[82:83], v[18:19]\n" \
  "v_pk_fma_f32 v[20:21], v[20:21], v[84:85], v[20:21]\n" \
  "v_pk_fma_f32 v[22:23], v[22:23], v[86:87], v[22:23]\n" \
  "v_exp_f32 v80, v56\n" \
  "v_exp_f32 v81, v57\n" \
  "v_exp_f32 v82, v58\n" \
  "v_exp_f32 v83, v59\n" \
  "v_exp_f32 v84, v60\n" \
  "v_exp_f32 v85, v61\n" \
  "v_exp_f32 v86, v62\n" \
  "v_exp_f32 v87, v63\n" \
  "s_nop 0\n" \
  "v_pk_fma_f32 v[8:9], v[8:9], v[80:81], v[8:9]\n" \
  "v_pk_fma_f32 v[10:11], v[10:11], v[82:83], v[10:11]\n" \
  "v_pk_fma_f32 v[12:13], v[12:13], v[84:85], v[12:13]\n" \
  "v_pk_fma_f32 v[14:15], v[14:15], v[86:87], v[14:15]\n" \
  "v_pk_fma_f32 v[8:9], v[56:57], v[24:25], v[8:9]\n" \
  "v_pk_fma_f32 v[10:11], v[58:59], v[26:27], v[10:11]\n" \
  "v_pk_fma_f32 v[12:13], v[60:61], v[28:29], v[12:13]\n" \
  "v_pk_fma_f32 v[14:15], v[62:63], v[30:31], v[14:15]\n" \
  "v_pk_fma_f32 v[24:25], v[24:25], v[80:81], v[24:25]\n" \
  "v_pk_fma_f32 v[26:27], v[26:27], v[82:83], v[26:27]\n" \
  "v_pk_fma_f32 v[28:29], v[28:29], v[84:85], v[28:29]\n" \
  "v_pk_fma_f32 v[30:31], v[30:31], v[86:87], v[30:31]\n" \
  "v_rcp_f32 v48, v16\n" \
  "v_rcp_f32 v49, v17\n" \
  "v_rcp_f32 v50, v18\n" \
  "v_rcp_f32 v51, v19\n" \
  "v_rcp_f32 v52, v20\n" \
  "v_rcp_f32 v53, v21\n" \
  "v_rcp_f32 v54, v22\n" \
  "v_rcp_f32 v55, v23\n" \
  "v_rcp_f32 v56, v24\n" \
  "v_rcp_f32 v57, v25\n" \
  "v_rcp_f32 v58, v26\n" \
  "v_rcp_f32 v59, v27\n" \
  "v_rcp_f32 v60, v28\n" \
  "v_rcp_f32 v61, v29\n" \
  "v_rcp_f32 v62, v30\n" \
  "v_rcp_f32 v63, v31\n" \
  "v_pk_mul_f32 v[122:123], v[16:17], v[0:1]\n" \
  "v_pk_fma_f32 v[122:123], v[18:19], v[2:3], v[122:123]\n" \
  "v_pk_fma_f32 v[122:123], v[20:21], v[4:5], v[122:123]\n" \
  "v_pk_fma_f32 v[122:123], v[22:23], v[6:7], v[122:123]\n" \
  "v_pk_fma_f32 v[122:123], v[24:25], v[8:9], v[122:123]\n" \
  "v_pk_fma_f32 v[122:123], v[26:27], v[10:11], v[122:123]\n" \
  "v_pk_fma_f32 v[122:123], v[28:29], v[12:13], v[122:123]\n" \
  "v_pk_fma_f32 v[122:123], v[30:31], v[14:15], v[122:123]\n" \
  "v_pk_mul_f32 v[0:1], v[0:1], v[48:49]\n" \
  "v_pk_mul_f32 v[2:3], v[2:3], v[50:51]\n" \
  "v_pk_mul_f32 v[4:5], v[4:5], v[52:53]\n" \
  "v_pk_mul_f32 v[6:7], v[6:7], v[54:55]\n" \
  "v_pk_mul_f32 v[8:9], v[8:9], v[56:57]\n" \
  "v_pk_mul_f32 v[10:11], v[10:11], v[58:59]\n" \
  "v_pk_mul_f32 v[12:13], v[12:13], v[60:61]\n" \
  "v_pk_mul_f32 v[14:15], v[14:15], v[62:63]\n" \
  "v_add_f32 v122, v122, v123\n" \
  "s_lshl_b32 s41, s40, 13\n" \
  "v_cvt_pk_bf16_f32 v64, v0, v1\n" \
  "v_cvt_pk_bf16_f32 v65, v2, v3\n" \
  "v_cvt_pk_bf16_f32 v66, v4, v5\n" \
  "v_cvt_pk_bf16_f32 v67, v6, v7\n" \
  "v_cvt_pk_bf16_f32 v68, v8, v9\n" \
  "v_cvt_pk_bf16_f32 v69, v10, v11\n" \
  "v_cvt_pk_bf16_f32 v70, v12, v13\n" \
  "v_cvt_pk_bf16_f32 v71, v14, v15\n" \
  "v_cmp_class_f32 vcc, v122, %[cls]\n" \
  "s_bitcmp1_b32 %[own], s40\n" \
  "s_cbranch_scc1 Lown%=\n" \
  "ds_bpermute_b32 v72, v120, v64\n" \
  "ds_bpermute_b32 v73, v120, v65\n" \
  "ds_bpermute_b32 v74, v120, v66\n" \
  "ds_bpermute_b32 v75, v120, v67\n" \
  "ds_bpermute_b32 v76, v120, v68\n" \
  "ds_bpermute_b32 v77, v120, v69\n" \
  "ds_bpermute_b32 v78, v120, v70\n" \
  "ds_bpermute_b32 v79, v120, v71\n" \
  "s_waitcnt lgkmcnt(4)\n" \
  "buffer_store_dwordx4 v[72:75], v124, %[rs], s41 offen sc1\n" \
  "s_waitcnt lgkmcnt(0)\n" \
  "buffer_store_dwordx4 v[76:79], v124, %[rs], s41 offen offset:1024 sc1\n" \
  "s_branch Lst%=\n" \
  "Lown%=:\n" \
  "s_lshr_b32 s41, s40, 3\n" \
  "s_lshl_b32 s41, s41, 4\n" \
  "s_and_b32 s42, s40, 1\n" \
  "s_add_u32 s41, s41, s42\n" \
  "s_lshl_b32 s41, s41, 8\n" \
  "v_add_u32 v122, s41, v126\n" \
  "ds_write_b128 v122, v[64:67]\n" \
  "ds_write_b128 v122, v[68:71] offset:2048\n" \
  "Lst%=:\n" \
  "s_cmp_lg_u64 vcc, 0\n" \
  "s_cselect_b32 s41, 1, 0\n" \
  "s_lshl_b32 s41, s41, s40\n" \
  "s_or_b32 %[bad], %[bad], s41\n" \
  "s_mov_b32 s40, s43\n" \
  "s_mov_b32 s43, 31\n" \
  "s_cmp_lg_u32 s40, 31\n" \
  "s_cbranch_scc1 Lgrp%=\n"

DI void put_s(u32x4 v, char* lds, __amdgpu_buffer_rsrc_t rsb, u32 g, int slice, int ck, int n) {
  if (((g >> 1) & 3) == (u32)slice && g < 24) *(u32x4*)(lds + L_OWN + (g >> 3) * 4096 + ck * 512 + (g & 1) * 256 + n * 16) = v;
  else __builtin_amdgcn_raw_buffer_store_b128(v, rsb, g * 8192 + slice * 2048 + ck * 256 + n * 16, 0, SC1);
}

extern "C" __global__ __launch_bounds__(1024, 4) void k2(const u16* __restrict__ ea, const int* __restrict__ src,
                                              const int* __restrict__ dst, const u16* __restrict__ w1,
                                              const u16* __restrict__ w2, const u16* __restrict__ b2,
                                              char* __restrict__ ws, u16* __restrict__ out) {
  extern __shared__ __attribute__((aligned(16))) char lds[];
  int tid = threadIdx.x, l = tid & 63;
  const int w = __builtin_amdgcn_readfirstlane(tid >> 6);
  const int slice = (blockIdx.x & 7) >> 1, bucket = (blockIdx.x & 1) * 32 + (blockIdx.x >> 3);
  const int nb0 = bucket * NPB, nval = min(NPB, NN - nb0);
  u32* deg = (u32*)(lds + L_DEG);
  u32x2* order = (u32x2*)(lds + L_ORD);
  u32* misc = (u32*)(lds + L_MISC);
  const char* tsl = ws + WS_T + (size_t)slice * 2 * NN * 128;
  const u32x4 sp = make_srd(tsl, 0, 2 * NN * 128);
  const u32 pf0 = (blockIdx.x >> 3) * 782;
  u32x4 rec[4];
  asm volatile("s_nop 4" ::: "memory");
  if (w < 4) touch((tid * NB + bucket) * 4, make_srd(ws + WS_O, 0, 256 * NB * 4), L_CN + w * 256);
  asm volatile("s_nop 4\n\tbuffer_load_dwordx4 %0, %2, %3, 0 offen\n\tbuffer_load_dwordx4 %1, %2, %3, %4 offen"
               : "=&v"(rec[0]), "=&v"(rec[1])
               : "v"(CELL(bucket, w * 16 + (l >> 3) * 2) * 128 + (l & 7) * 16), "s"(make_srd(ws + WS_R, 0, NCELL * 128)), "s"(CELL(0, 1) * 128) : "memory");
  u32 w4[4] = {0u, 0u, 0u, 0u};
  if (w == 15) {
    const int rg = (l & 3) + 4 * ((l & 31) >> 3);
    asm volatile("global_load_ushort %0, %4, off\n\tglobal_load_ushort %1, %4, off offset:16\n\tglobal_load_ushort %2, %4, off offset:32\n\tglobal_load_ushort %3, %4, off offset:48"
                 : "=&v"(w4[0]), "=&v"(w4[1]), "=&v"(w4[2]), "=&v"(w4[3]) : "v"(w1 + 256 * 256 + slice * 64 + 32 * (rg >> 3) + (rg & 7)) : "memory");
  }
  __builtin_amdgcn_sched_barrier(0);
  if (tid < 464) deg[tid] = tid < 400 ? L_LIST + tid * LST * 4 : L_W2 + (tid - 400) * 4;
  if (tid < 416) order[tid] = u32x2{(u32)(L_LIST + NPB * LST * 4), 100000u};
  if (tid < 256) ((u32*)(lds + L_WC))[tid] = 0;
  if (tid < 4) misc[tid] = 0;
  __builtin_amdgcn_sched_barrier(0);
  u32x4 ida;
  u32 six;
  {
    const int r = l & 31, rg = (r & 3) + 4 * (r >> 3), dw = 2 * ((r >> 2) & 1) + ((rg >> 2) & 1);
#pragma unroll
    for (int j = 0; j < 4; ++j) ida[j] = ((l >> 5) == (rg >> 3) && j == dw) ? 0xBF80u : 0u;
    six = ((rg & 3) | (((rg & 3) ^ 1) << 2)) * 0x1111u;
    asm volatile("" : "+v"(ida), "+v"(six));
  }
  if (w < 4) asm volatile("s_waitcnt vmcnt(2)" ::: "memory");
  lds_barrier();
  {
    const u32 cn = *(const u32*)(lds + L_CN + w * 64 + (l >> 2) * 4);
    const u32x4 r1 = make_srd(ws + WS_R1, 0, NCELL * 256);
    const u32 o1 = CELL(0, w * 16 + (l >> 2)) * 256 + (bucket >> 4) * 4096 + ((cn >> 16) + (l & 3)) * 16;
    const int left = (int)(cn & 0xFFFFu) - 24 - 3 * (l & 3);
    asm volatile("buffer_load_dwordx4 %0, %2, %4, 0 offen\n\tbuffer_load_dwordx4 %1, %3, %4, 0 offen offset:64" : "=&v"(rec[2]), "=&v"(rec[3])
                 : "v"(left > 0 ? o1 : OOB), "v"(left > 12 ? o1 : OOB), "s"(r1) : "memory");
    const bool bad = left > 48;
    u32 at[12];
#define K2_COUNT(i) _Pragma("unroll") for (int j = 0; j < 3; ++j) at[3 * (i) + j] = atomicAdd(&deg[(rec[i][3] >> (10 * j)) & 0x3FFu], 4u);
#define K2_PUT(i) _Pragma("unroll") for (int j = 0; j < 3; ++j) *(LAS u32*)(size_t)at[3 * (i) + j] = rec[i][j];
    if (w == 15) asm volatile("s_waitcnt vmcnt(7)" ::: "memory"); else asm volatile("s_waitcnt vmcnt(3)" ::: "memory");
    asm volatile("" : "+v"(rec[0]) :: "memory");
    K2_COUNT(0);
    __builtin_amdgcn_sched_barrier(0);
    if (w == 15) asm volatile("s_waitcnt vmcnt(6)" ::: "memory"); else asm volatile("s_waitcnt vmcnt(2)" ::: "memory");
    asm volatile("" : "+v"(rec[1]) :: "memory");
    K2_COUNT(1);
    __builtin_amdgcn_sched_barrier(0);
    K2_PUT(0);
    K2_PUT(1);
    __builtin_amdgcn_sched_barrier(0);
    asm volatile("s_waitcnt vmcnt(0)" : "+v"(rec[2]), "+v"(rec[3]) :: "memory");
    if (w >= 7 && w < 12) touch((pf0 + (w - 7) * 64 + l) * 128, sp, L_DUM);
    if (left > 0) {
      K2_COUNT(2);
      if (left > 12) { K2_COUNT(3); }
      K2_PUT(2);
      if (left > 12) { K2_PUT(3); }
    }
#pragma unroll 1
    for (int k = 2; __builtin_expect(k < 4 && __any(left > 12 * k), 0); ++k) {
      asm volatile("buffer_load_dwordx4 %0, %1, %2, 0 offen\n\ts_waitcnt vmcnt(0)" : "=&v"(rec[0]) : "v"(left > 12 * k ? o1 + 64 * k : OOB), "s"(r1) : "memory");
      if (left > 12 * k) {
        K2_COUNT(0);
        K2_PUT(0);
      }
    }
    if (bad) misc[1] = 1;
    if (w == 15) {
      asm volatile("" : "+v"(w4[0]), "+v"(w4[1]), "+v"(w4[2]), "+v"(w4[3]));
      u32x2 a = {pk_bf16(bf2f(w4[0]) * -LOG2E, bf2f(w4[1]) * -LOG2E), pk_bf16(bf2f(w4[2]) * -LOG2E, bf2f(w4[3]) * -LOG2E)};
      if (((l >> 2) & 1) != (l >> 5)) a = u32x2{0u, 0u};
      *(u32x2*)(lds + L_AW + l * 8) = a;
    }
  }
  lds_barrier();
  u32 myd = deg[min(tid, 463)], myr = 0;
  const u32x2 aw = *(const u32x2*)(lds + L_AW + l * 8);
  __builtin_amdgcn_sched_barrier(0);
  if (w < 7) {
    myd = tid < nval ? (myd - (L_LIST + tid * LST * 4)) >> 2 : 0u;
    deg[tid] = myd;
    if (myd > LST) misc[1] = 1;
    myd = min(myd, 63u);
    const u64 m0 = __builtin_amdgcn_ballot_w64(tid < nval);
    u32 ml = (u32)m0, mh = (u32)(m0 >> 32);
    int bit[6];
    u64 bb[6];
#pragma unroll
    for (int k = 0; k < 6; ++k) bit[k] = __builtin_amdgcn_sbfe((int)myd, k, 1);
#pragma unroll
    for (int k = 0; k < 6; ++k) bb[k] = __builtin_amdgcn_ballot_w64(bit[k] < 0);
#pragma unroll
    for (int k = 0; k < 6; ++k) {
      ml = __builtin_amdgcn_bitop3_b32(ml, (u32)bb[k], (u32)bit[k], 0x90);
      mh = __builtin_amdgcn_bitop3_b32(mh, (u32)(bb[k] >> 32), (u32)bit[k], 0x90);
    }
    *(u16*)(lds + (tid < nval ? L_WC + myd * 16 + w * 2 : L_DUM)) = (u16)(__builtin_popcount(ml) + __builtin_popcount(mh));
    myr = __builtin_amdgcn_mbcnt_hi(mh, __builtin_amdgcn_mbcnt_lo(ml, 0u));
  }
  lds_barrier();
  const u32 mf = misc[1];
  if (w < 7) {
    const u32x4 ra = *(const u32x4*)(lds + L_WC + (63 - l) * 16), rb = *(const u32x4*)(lds + L_WC + myd * 16);
    __builtin_amdgcn_sched_barrier(0);
    u32 own = ra[0] + ra[1] + ra[2] + ra[3], pre = 0;
    own = (own & 0xFFFFu) + (own >> 16);
#pragma unroll
    for (int j = 0; j < 4; ++j) pre += rb[j] & (w > 2 * j + 1 ? 0xFFFFFFFFu : w > 2 * j ? 0xFFFFu : 0u);
    const u32 st = __builtin_amdgcn_ds_bpermute((63 - myd) * 4, scan64(own) - own) + (pre & 0xFFFFu) + (pre >> 16);
    const u32 ps = st + myr, rk = ps >> 4;
    const u32 gn = rk <= 14 ? rk + 8 : rk <= 16 ? rk - 15 : rk == 17 ? 23 : rk <= 23 ? rk - 16 : 24;
    *(u32x2*)(lds + (tid < nval ? L_ORD + (gn * 16 + (ps & 15)) * 8 : L_DUM)) = u32x2{(u32)(L_LIST + tid * LST * 4) | (myd << 20), (u32)(nb0 + tid)};
  }
  const bool redo = __builtin_amdgcn_readfirstlane(mf) != 0;
  lds_barrier();
  char* sg = ws + WS_S + (size_t)bucket * 25 * 8192;
  const u32 gf = (u32)((w < 8 ? 0x1805010706000304ull : 0x1f1f1f1f1f1f1f02ull) >> (8 * (w & 7))) & 31u;
  const u32 gl = (u32)((w < 8 ? 0x1311121016150f14ull : 0x0c0b0d0e08090a17ull) >> (8 * (w & 7))) & 31u;
  const bool two = w < 9;
  const u32 g0 = __builtin_amdgcn_readfirstlane(two ? gf : gl), g1 = __builtin_amdgcn_readfirstlane(two ? gl : 31u);
  const __amdgpu_buffer_rsrc_t rsb = make_rsrc(sg, 25 * 8192);
  u32* flag = (u32*)(ws + WS_F + bucket * 256);
  bool told = false;
  if (__builtin_expect(!redo, 1)) {
    const u32x4 rt = make_srd(tsl, 128, 2 * NN), rs = make_srd(sg + slice * 2048, 0, 25 * 8192 - slice * 2048);
    u32 bad = 0;
    asm volatile(K2_MAIN_ASM
                 : [bad] "+s"(bad)
                 : [rt] "s"(rt), [rs] "s"(rs), [g0] "s"(g0), [g1] "s"(g1), [cls] "s"(0x207), [fl] "s"(flag), [own] "s"(0x030303u << (2 * slice)), [nn] "i"(NN),
                   "{v[104:107]}"(ida), "{v[108:109]}"(aw), "{v110}"(six), "{v119}"((l & 3) * 16), "{v121}"(L_ORD + (l >> 2) * 8),
                   "{v120}"((l & 15) * 16 + (l >> 4) * 4), "{v124}"(l * 16), "{v125}"((l & 3) * 4),
                   "{v126}"(L_OWN + (l & 2) * 512 + (l & 1) * 512 + (l >> 2) * 16)
                 : "memory", "vcc", "scc", "s40", "s41", "s42", "s43", "v0", "v1", "v2", "v3", "v4", "v5", "v6", "v7", "v8", "v9", "v10", "v11", "v12", "v13", "v14", "v15", "v16", "v17", "v18", "v19", "v20", "v21", "v22", "v23", "v24", "v25", "v26", "v27", "v28", "v29", "v30", "v31", "v32", "v33", "v34", "v35", "v36", "v37", "v38", "v39", "v40", "v41", "v42", "v43", "v44", "v45", "v46", "v47", "v48", "v49", "v50", "v51", "v52", "v53", "v54", "v55", "v56", "v57", "v58", "v59", "v60", "v61", "v62", "v63", "v64", "v65", "v66", "v67", "v68", "v69", "v70", "v71", "v72", "v73", "v74", "v75", "v76", "v77", "v78", "v79", "v80", "v81", "v82", "v83", "v84", "v85", "v86", "v87", "v88", "v89", "v90", "v91", "v92", "v93", "v94", "v95", "v96", "v97", "v98", "v99", "v100", "v101", "v102", "v103", "v111", "v112", "v113", "v114", "v115", "v116", "v117", "v118", "v122", "v123");
    told = !((bad >> g0) & 1);
    while (__builtin_expect(bad != 0, 0)) {
      const int g = __builtin_ctz(bad);
      bad &= bad - 1;
      l = __builtin_amdgcn_mbcnt_hi(~0u, __builtin_amdgcn_mbcnt_lo(~0u, 0u));
      const u32x2 oe = order[g * 16 + (l >> 2)];
      if (oe[1] < (u32)NN) {
        const u32* lp = (const u32*)(lds + (oe[0] & 0xFFFFFu));
        const int len = oe[0] >> 20;
#pragma unroll 1
        for (int i = 0; i < 2; ++i) {
          const int co = 64 * i + (l & 3) * 16;
          const u32x4 qv = *(const u32x4*)(tsl + (size_t)(NN + oe[1]) * 128 + co);
          const u32x4 wv = *(const u32x4*)((const char*)(w1 + 256 * 256 + slice * 64) + co);
          float s[8];
#pragma unroll
          for (int j = 0; j < 8; ++j) s[j] = 0.f;
          for (int k = 0; k < len; ++k) {
            const u32 en = lp[k];
            const u32x4 pv = *(const u32x4*)(tsl + (size_t)(en >> 16) * 128 + co);
            const float e = bf2f(en & 0xFFFFu) * -LOG2E;
#pragma unroll
            for (int j = 0; j < 8; ++j) {
              const int sh = 16 * (j & 1);
              const float t = bf2f((qv[j >> 1] >> sh) & 0xFFFFu) - bf2f((pv[j >> 1] >> sh) & 0xFFFFu) + e * bf2f((wv[j >> 1] >> sh) & 0xFFFFu);
              s[j] += t * __builtin_amdgcn_rcpf(1.f + __builtin_amdgcn_exp2f(t));
            }
          }
          put_s(u32x4{pk_bf16(s[0], s[1]), pk_bf16(s[2], s[3]), pk_bf16(s[4], s[5]), pk_bf16(s[6], s[7])}, lds, rsb, g, slice, 4 * i + (l & 3), l >> 2);
        }
      }
    }
  } else {
    float* sum = (float*)(lds + L_LIST);
    for (int i = tid; i < NPB * 64; i += 1024) sum[i] = 0.f;
    if (tid < 400) { deg[tid] = 0; order[tid] = u32x2{0u, tid < nval ? (u32)(nb0 + tid) : 100000u}; }
    __syncthreads();
    const float wv = bf2f(w1[256 * 256 + slice * 64 + l]) * -LOG2E;
    const u16* tp = (const u16*)tsl;
    for (int e0 = w * 64; e0 < NE; e0 += 1024) {
      const u32 dl = (u32)(dst[e0 + l] - nb0);
      u64 m = __ballot(dl < (u32)nval);
      while (m) {
        const int j = __ffsll((long long)m) - 1;
        m &= m - 1;
        const u32 d = __builtin_amdgcn_readlane(dl, j);
        const int s = src[e0 + j];
        const float t = bf2f(tp[(NN + nb0 + d) * 64 + l]) - bf2f(tp[s * 64 + l]) + bf2f(ea[e0 + j]) * wv;
        atomicAdd(&sum[d * 64 + l], t * __builtin_amdgcn_rcpf(1.f + __builtin_amdgcn_exp2f(t)));
        if (l == 0) atomicAdd(&deg[d], 1u);
      }
    }
    __syncthreads();
#pragma unroll 1
    for (int k = 0; k < 4; ++k) {
      const u32 g = (k >> 1) ? g1 : g0, n = min(g * 16 + (l & 15), (u32)NPB - 1);
      const float* sp = sum + n * 64 + 32 * (k & 1) + (l >> 4) * 8;
      put_s(u32x4{pk_bf16(sp[0], sp[1]), pk_bf16(sp[2], sp[3]), pk_bf16(sp[4], sp[5]), pk_bf16(sp[6], sp[7])}, lds, rsb, g, slice, 4 * (k & 1) + (l >> 4), l & 15);
    }
    __syncthreads();
  }
  l = __builtin_amdgcn_mbcnt_hi(~0u, __builtin_amdgcn_mbcnt_lo(~0u, 0u));
  u32 seen = 0;
  if (w < 8) {
    const u32x4 sw = make_srd(w2, 0, 65536);
    const u32 vw = (l >> 4) * 256 + (((l & 15) ^ ((l >> 4) << 2)) << 4);
    asm volatile("s_nop 4\n\tglobal_load_dword %0, %1, %2 offset:128 sc1" : "=v"(seen) : "v"(0), "s"(flag) : "memory");
    if (w == 0) dma16(l * 16, make_srd(b2, 0, 256), 0, L_CN);
#pragma unroll
    for (int i = 0; i < 8; ++i) dma16(vw, sw, (w * 8 + i) * 1024, L_W2 + (w * 8 + i) * 1024);
  }
  const int c = l & 31, hf = l >> 5, dt = w < 4 ? slice : w & 3, item = w < 4 ? 12 : slice + 4 * (w >> 2) - 4;
  const u32 so = item * 16384 + (c & 16) * 512 + hf * 256 + (c & 15) * 16;
  const u32x4 ssd = make_srd(sg, 0, 25 * 8192);
  u32 sf[4];
#pragma unroll
  for (int k = 0; k < 4; ++k) sf[k] = w < 4 ? w * 2048 + k * 512 : ((slice + 1 + ((3 * dt + k) >> 2)) & 3) * 2048 + ((3 * dt + k) & 3) * 512;
  asm volatile("" : "+s"(sf[0]), "+s"(sf[1]), "+s"(sf[2]), "+s"(sf[3]));
  __builtin_amdgcn_sched_barrier(0);
  u32x4 sb0, sb1, sb2, sb3;
#define K2_FETCH asm volatile("s_nop 4\n\tbuffer_load_dwordx4 %0, %3, %4, %5 offen sc1\n\tbuffer_load_dwordx4 %1, %3, %4, %6 offen sc1\n\tbuffer_load_dwordx4 %2, %3, %4, %7 offen sc1" \
                              : "=&v"(sb0), "=&v"(sb1), "=&v"(sb2) : "v"(so), "s"(ssd), "s"(sf[0]), "s"(sf[1]), "s"(sf[2]) : "memory")
  const u32 node = order[item * 32 + c][1];
  const float dg = w == 0 || w >= 4 ? (float)deg[min(node - nb0, 400u)] * -LOG2E : 0.f;
  u32 sn[2];
#pragma unroll
  for (int u = 0; u < 2; ++u) sn[u] = __builtin_amdgcn_ds_bpermute((16 * u + (l >> 2)) * 4, node) * 256 + dt * 64 + (l & 3) * 16;
  asm volatile("s_waitcnt vmcnt(0)" : "+v"(sn[0]), "+v"(sn[1]), "+v"(seen) :: "memory");
  if (__builtin_expect(two && !told, 0) && l == 0) __hip_atomic_fetch_add(flag + 32, 1u, __ATOMIC_RELAXED, __HIP_MEMORY_SCOPE_AGENT);
  if (w < 8) {
#pragma unroll 1
    for (int spin = 0; __builtin_expect(seen < 36u && spin < (1 << 20), 0); ++spin) {
      __builtin_amdgcn_s_sleep(4);
      seen = __hip_atomic_load(flag + 32, __ATOMIC_RELAXED, __HIP_MEMORY_SCOPE_AGENT);
    }
    K2_FETCH;
    if (w < 4) asm volatile("buffer_load_dwordx4 %0, %1, %2, %3 offen sc1" : "=v"(sb3) : "v"(so), "s"(ssd), "s"(sf[3]) : "memory");
  }
  lds_barrier();
  asm volatile("s_waitcnt vmcnt(0)" : "+v"(sb0), "+v"(sb1), "+v"(sb2), "+v"(sb3) :: "memory");
  if (w == 8 && l == 0) __hip_atomic_fetch_add(flag, 1u, __ATOMIC_RELAXED, __HIP_MEMORY_SCOPE_AGENT);
  u32x4* sq = (u32x4*)(lds + L_LIST + (w - 4) * 3072) + l;
  if (w >= 4 && w < 8) {
    sq[0] = sb0;
    sq[64] = sb1;
    sq[128] = sb2;
  }
  const u32x4 bb0 = *(const u32x4*)(lds + L_CN + dt * 64 + hf * 32), bb1 = *(const u32x4*)(lds + L_CN + dt * 64 + hf * 32 + 16);
  f32x16 acc;
#pragma unroll
  for (int j = 0; j < 8; ++j) {
    const u32 bb = j < 4 ? bb0[j & 3] : bb1[j & 3];
    acc[2 * j] = dg * bf2f(bb & 0xFFFFu);
    acc[2 * j + 1] = dg * __uint_as_float(bb & 0xFFFF0000u);
  }
  const int q = (l >> 2) & 3, r4 = 4 * ((l >> 4) & 1) + (l & 3), hc = 4 * (r4 & 1) + (r4 >> 1);
  const u32 wa = L_W2 + (8 * hf + q) * 256 + (((4 * dt + (hc >> 1)) ^ (q << 2)) << 4) + (hc & 1) * 8;
  if (w < 4) {
    bf16x8 af[4];
#pragma unroll
    for (int k = 0; k < 4; ++k) af[k] = tr_frag(wa + w * 16384 + k * 4096, wa + w * 16384 + k * 4096 + 1024);
    acc = MFMA(af[0], __builtin_bit_cast(bf16x8, sb0), acc);
    acc = MFMA(af[1], __builtin_bit_cast(bf16x8, sb1), acc);
    acc = MFMA(af[2], __builtin_bit_cast(bf16x8, sb2), acc);
    acc = MFMA(af[3], __builtin_bit_cast(bf16x8, sb3), acc);
    f32x4* pp = (f32x4*)(lds + L_LIST + 36864) + l;
    if (w) {
#pragma unroll
      for (int u = 0; u < 4; ++u) pp[(w * 4 + u - 4) * 64] = f32x4{acc[4 * u], acc[4 * u + 1], acc[4 * u + 2], acc[4 * u + 3]};
    }
    if (w == 3) {
#pragma unroll 1
      for (int spin = 0; spin < (1 << 20); ++spin) {
        if (__hip_atomic_load(flag, __ATOMIC_RELAXED, __HIP_MEMORY_SCOPE_AGENT) >= 4u) break;
        __builtin_amdgcn_s_sleep(1);
      }
    }
    lds_barrier();
    if (w) return;
#pragma unroll
    for (int u = 0; u < 12; ++u) {
      const f32x4 t = pp[u * 64];
#pragma unroll
      for (int j = 0; j < 4; ++j) acc[4 * (u & 3) + j] += t[j];
    }
  } else {
    bf16x8 af[12];
    {
      bf16x8 ao[4], sw[4];
#pragma unroll
      for (int k = 0; k < 4; ++k) {
        ao[k] = tr_frag(wa + slice * 16384 + k * 4096, wa + slice * 16384 + k * 4096 + 1024);
        sw[k] = *(const bf16x8*)(lds + L_OWN + ((w >> 2) - 1) * 4096 + k * 1024 + l * 16);
      }
      __builtin_amdgcn_sched_barrier(0);
#pragma unroll
      for (int k = 0; k < 4; ++k) acc = MFMA(ao[k], sw[k], acc);
      asm volatile("" : "+v"(acc));
#pragma unroll
      for (int k = 0; k < 12; ++k) {
        const u32 ak = wa + ((slice + 1 + (k >> 2)) & 3) * 16384 + (k & 3) * 4096;
        af[k] = tr_frag(ak, ak + 1024);
      }
    }
    lds_barrier();
    if (w >= 8) {
      u32x4 t[3];
#pragma unroll
      for (int k = 0; k < 3; ++k) t[k] = __builtin_amdgcn_raw_buffer_load_b128(rsb, so, sf[k], SC1);
#pragma unroll
      for (int k = 0; k < 3; ++k) sq[64 * k] = t[k];
      {
        asm volatile("s_waitcnt lgkmcnt(0)" ::: "memory");
        if (l == 0) atomicAdd(&misc[w >> 2], 1u);
        while (*(volatile LAS u32*)(size_t)(L_MISC + 4 * (w >> 2)) < 4u) {}
        asm volatile("" ::: "memory");
      }
    }
    bf16x8 sb[6];
    const char* sp = lds + L_LIST + ((w >> 2) - 1) * 12288 + l * 16;
#pragma unroll
    for (int k = 0; k < 6; ++k) sb[k] = *(const bf16x8*)(sp + k * 1024);
    __builtin_amdgcn_sched_barrier(0);
#pragma unroll
    for (int k = 0; k < 6; ++k) {
      acc = MFMA(af[k], sb[k], acc);
      sb[k] = *(const bf16x8*)(sp + (6 + k) * 1024);
      __builtin_amdgcn_sched_barrier(0);
    }
#pragma unroll
    for (int k = 0; k < 6; ++k) acc = MFMA(af[6 + k], sb[k], acc);
  }
  float nl = -LN2;
  asm volatile("" : "+v"(nl));
  char* stg = lds + L_W2 + w * 2048;
#pragma unroll
  for (int u = 0; u < 2; ++u) {
    u32x4 o;
#pragma unroll
    for (int j = 0; j < 4; ++j) o[j] = pk_bf16(mul1(acc[8 * u + 2 * j], nl), mul1(acc[8 * u + 2 * j + 1], nl));
    *(u32x4*)(stg + c * 64 + (((2 * hf + u) ^ ((c >> 1) & 3)) << 4)) = o;
  }
  const __amdgpu_buffer_rsrc_t ro = make_rsrc(out, NN * 256);
#pragma unroll
  for (int u = 0; u < 2; ++u) {
    const int nd = 16 * u + (l >> 2);
    const u32x4 o = *(const u32x4*)(stg + nd * 64 + (((l & 3) ^ ((nd >> 1) & 3)) << 4));
    __builtin_amdgcn_raw_buffer_store_b128(o, ro, sn[u], 0, SC1);
  }
  asm volatile("mpnn_end:");
}

extern "C" void kernel_launch(void* const* d_in, const int* in_sizes, int n_in, void* d_out, int out_size,
                              void* d_ws, size_t ws_size, hipStream_t stream) {
  if (n_in != 8 || in_sizes[0] != NN * 128 || in_sizes[2] != NE || out_size != NN * 128 || ws_size < WS_F + NB * 256) return;
  const u16* x = (const u16*)d_in[0];
  const u16* ea = (const u16*)d_in[1];
  const int* src = (const int*)d_in[2];
  const int* dst = (const int*)d_in[3];
  const u16* w1 = (const u16*)d_in[4];
  const u16* b1 = (const u16*)d_in[5];
  const u16* w2 = (const u16*)d_in[6];
  const u16* b2 = (const u16*)d_in[7];
  char* ws = (char*)d_ws;
  (void)hipFuncSetAttribute((const void*)k1, hipFuncAttributeMaxDynamicSharedMemorySize, K1_BYTES);
  (void)hipFuncSetAttribute((const void*)k2, hipFuncAttributeMaxDynamicSharedMemorySize, L2_BYTES);
  k1<<<256, 768, K1_BYTES, stream>>>(x, ea, src, dst, w1, b1, ws);
  k2<<<256, 1024, L2_BYTES, stream>>>(ea, src, dst, w1, w2, b2, ws, (u16*)d_out);
}
```
